```python
import torch

def _softmax(x, axis=-1):
    return torch.softmax(x, dim=axis)
def _gelu(x):
    return torch.nn.functional.gelu(x, approximate="tanh")
def _silu(x):
    return torch.nn.functional.silu(x)
def _sigmoid(x):
    return torch.sigmoid(x)
def _layernorm(x, axis=-1, eps=1e-5):
    m = x.mean(dim=axis, keepdim=True)
    v = x.var(dim=axis, keepdim=True, unbiased=False)
    return (x - m) / torch.sqrt(v + eps)
def _scatter_add(tgt, idx, src, axis=0):
    return tgt.clone().index_add_(axis, idx, src)
def _segment_sum(src, seg, n):
    out = torch.zeros((n,) + src.shape[1:], dtype=src.dtype)
    return out.index_add_(0, seg, src)
def _pad(x, pw):
    flat = tuple(v for pair in reversed(pw) for v in pair)
    return torch.nn.functional.pad(x, flat)

kernel_name = 'interleave_layers__linear_kernel_hedgehog__sliding_block__prompt'

def setup_inputs():

    _w = (lambda a: (lambda t: t.to(torch.bfloat16) if t.is_floating_point() else t)(torch.as_tensor(a)))
    def _A_setup():
        B, S, D, H = 1, 2048, 576, 32
        Dh = D // H
        return {
            "x": _w((lambda *s: torch.randn(*s, dtype=torch.bfloat16) * 0.05)(B, S, D)),
            "wqkv": _w((lambda *s: torch.randn(*s, dtype=torch.bfloat16) * 0.2)(D, 3 * D)),
            "w_phi": _w((lambda *s: torch.randn(*s, dtype=torch.bfloat16) * 0.2)(H, Dh, Dh) * (Dh ** -0.5)),
            "wo": _w((lambda *s: torch.randn(*s, dtype=torch.bfloat16) * 0.05)(D, D)),
        }
    _A_in = _A_setup()

    def _B_setup():
        B, S, D = 1, 2048, 576
        return {
            "x": _w((lambda *s: torch.randn(*s, dtype=torch.bfloat16) * 0.05)(B, S, D)),
            "wqkv": _w((lambda *s: torch.randn(*s, dtype=torch.bfloat16) * 0.1)(D, 3 * D)),
            "wo": _w((lambda *s: torch.randn(*s, dtype=torch.bfloat16) * 0.05)(D, D)),
        }
    _B_in = _B_setup()

    return {
        "A_x": _A_in["x"],
        "A_wqkv": _A_in["wqkv"],
        "A_w_phi": _A_in["w_phi"],
        "A_wo": _A_in["wo"],
        "B_x": _B_in["x"],
        "B_wqkv": _B_in["wqkv"],
        "B_wo": _B_in["wo"],
    }

def reference(A_x, A_wqkv, A_w_phi, A_wo, B_x, B_wqkv, B_wo):
    def _A_ref(x, wqkv, w_phi, wo):
        B, S, D = x.shape
        H = 32; Dh = D // H
        qkv = (lambda x, *d: x.permute(*d))((x @ wqkv).reshape(B, S, 3, H, Dh), 2, 0, 3, 1, 4)
        q, k, v = qkv[0], qkv[1], qkv[2]
        pq = torch.einsum("bhsd,hdf->bhsf", q, w_phi)
        pk = torch.einsum("bhsd,hdf->bhsf", k, w_phi)
        phi_q = torch.concatenate([_softmax(pq, axis=-1), _softmax(-pq, axis=-1)], axis=-1)
        phi_k = torch.concatenate([_softmax(pk, axis=-1), _softmax(-pk, axis=-1)], axis=-1)
        kv = torch.einsum("bhsf,bhsd->bhfd", phi_k, v)
        z  = torch.einsum("bhsf->bhf", phi_k)
        num = torch.einsum("bhqf,bhfd->bhqd", phi_q, kv)
        den = torch.einsum("bhqf,bhf->bhq", phi_q, z)[..., None] + 1e-6
        o = num / den
        return (lambda x, *d: x.permute(*d))(o, 0, 2, 1, 3).reshape(B, S, D) @ wo

    def _B_ref(x, wqkv, wo):
        B, S, D = x.shape
        H = 32; Dh = D // H; W = 256
        qkv = (lambda x, *d: x.permute(*d))((x @ wqkv).reshape(B, S, 3, H, Dh), 2, 0, 3, 1, 4)
        q, k, v = qkv[0], qkv[1], qkv[2]
        scores = torch.einsum("bhqd,bhkd->bhqk", q, k) * (Dh ** -0.5)
        idx = torch.arange(S)
        mask = (idx[:, None] - idx[None, :] >= 0) & (idx[:, None] - idx[None, :] < W)
        scores = torch.where(mask[None, None], scores, -1e9)
        p = _softmax(scores, axis=-1)
        o = torch.einsum("bhqk,bhkd->bhqd", p, v)
        return (lambda x, *d: x.permute(*d))(o, 0, 2, 1, 3).reshape(B, S, D) @ wo

    x = A_x
    x = x + _A_ref(_layernorm(x, axis=-1), A_wqkv, A_w_phi, A_wo)
    x = x + _B_ref(_layernorm(x, axis=-1), B_wqkv, B_wo)
    x = x + _A_ref(_layernorm(x, axis=-1), A_wqkv, A_w_phi, A_wo)
    x = x + _B_ref(_layernorm(x, axis=-1), B_wqkv, B_wo)
    return x
```

```cpp
#include <hip/hip_runtime.h>
#include <hip/hip_bf16.h>
typedef unsigned short u16;
__device__ __forceinline__ float b2f(u16 v){ return __uint_as_float(((unsigned)v)<<16); }
__device__ __forceinline__ u16 f2b(float f){ unsigned u=__float_as_uint(f); u += 0x7FFF + ((u>>16)&1); return (u16)(u>>16);}
#define S 2048
#define D 576
#define H 32
#define DH 18
__global__ void k_ln(const u16* x, u16* y){
  int row=blockIdx.x; int l=threadIdx.x; float v[9]; float s=0;
  for(int i=0;i<9;i++){ v[i]=b2f(x[row*D+l+64*i]); s+=v[i]; }
  for(int m=32;m;m>>=1) s+=__shfl_xor(s,m);
  float mean=s/D; float q=0; for(int i=0;i<9;i++){ float d=v[i]-mean; q+=d*d; }
  for(int m=32;m;m>>=1) q+=__shfl_xor(q,m);
  float r=rsqrtf(q/D+1e-5f);
  for(int i=0;i<9;i++) y[row*D+l+64*i]=f2b((v[i]-mean)*r);
}
__global__ void k_gemm(const u16* A,const u16* B,u16* C,const u16* R,int N,int K){
  __shared__ float As[16][65]; __shared__ float Bs[64][65];
  int tx=threadIdx.x&63, ty=threadIdx.x>>6;
  int n=blockIdx.x*64+tx; int m0=blockIdx.y*16;
  float acc[4]={0,0,0,0};
  for(int k0=0;k0<K;k0+=64){
    for(int i=threadIdx.x;i<16*64;i+=256){ As[i>>6][i&63]=b2f(A[(m0+(i>>6))*K+k0+(i&63)]); }
    for(int i=threadIdx.x;i<64*64;i+=256){ Bs[i>>6][i&63]=b2f(B[(k0+(i>>6))*N+blockIdx.x*64+(i&63)]); }
    __syncthreads();
    for(int k=0;k<64;k++){ float b=Bs[k][tx]; for(int j=0;j<4;j++) acc[j]+=As[ty*4+j][k]*b; }
    __syncthreads();
  }
  for(int j=0;j<4;j++){ int m=m0+ty*4+j; float v=b2f(f2b(acc[j])); if(R) v+=b2f(R[m*N+n]); C[m*N+n]=f2b(v);}
}
__global__ void __launch_bounds__(256) k_phi(const u16* qkv,const u16* wphi,float* phi){
  int t=blockIdx.x*blockDim.x+threadIdx.x; int which=t/(S*H); int r=t%(S*H); int s=r/H,h=r%H;
  float q[DH],p[DH]; for(int d=0;d<DH;d++) q[d]=b2f(qkv[s*3*D+which*D+h*DH+d]);
  float mx=-1e30f,mn=1e30f;
  _Pragma("unroll") for(int f=0;f<DH;f++){ float a=0; _Pragma("unroll") for(int d=0;d<DH;d++) a+=q[d]*b2f(wphi[(h*DH+d)*DH+f]); p[f]=a; mx=fmaxf(mx,a); mn=fminf(mn,a);}
  float s1=0,s2=0; float e1[DH],e2[DH];
  _Pragma("unroll") for(int f=0;f<DH;f++){ e1[f]=__expf(p[f]-mx); e2[f]=__expf(mn-p[f]); s1+=e1[f]; s2+=e2[f]; }
  float* o=phi+((size_t)(which*S+s)*H+h)*36;
  _Pragma("unroll") for(int f=0;f<DH;f++){ o[f]=e1[f]/s1; o[18+f]=e2[f]/s2; }
}
__global__ void k_kv(const float* phi,const u16* qkv,float* kv){
  int h=blockIdx.x; int t=threadIdx.x; if(t>=36*19) return; int f=t/19,d=t%19; float a=0;
  for(int s=0;s<S;s++){ float p=phi[((size_t)(S+s)*H+h)*36+f]; float v=d<18? b2f(qkv[s*3*D+2*D+h*DH+d]):1.f; a+=p*v; }
  kv[(h*36+f)*19+d]=a;
}
__global__ void k_oA(const float* phi,const float* kv,u16* o){
  int t=blockIdx.x*blockDim.x+threadIdx.x; int s=t/H,h=t%H; const float* p=phi+((size_t)s*H+h)*36;
  float den=1e-6f; for(int f=0;f<36;f++) den+=p[f]*kv[(h*36+f)*19+18];
  for(int d=0;d<18;d++){ float a=0; for(int f=0;f<36;f++) a+=p[f]*kv[(h*36+f)*19+d]; o[s*D+h*DH+d]=f2b(a/den);}
}
__global__ void k_att(const u16* qkv,u16* o){
  int t=blockIdx.x*blockDim.x+threadIdx.x; int h=t/S,s=t%S; float q[DH]; for(int d=0;d<DH;d++) q[d]=b2f(qkv[s*3*D+h*DH+d]);
  float sc=rsqrtf(18.f); int lo=s-255<0?0:s-255; float mx=-1e30f;
  for(int j=lo;j<=s;j++){ float a=0; for(int d=0;d<DH;d++) a+=q[d]*b2f(qkv[j*3*D+D+h*DH+d]); mx=fmaxf(mx,a*sc);}
  float acc[DH]; for(int d=0;d<DH;d++) acc[d]=0; float l=0;
  for(int j=lo;j<=s;j++){ float a=0; for(int d=0;d<DH;d++) a+=q[d]*b2f(qkv[j*3*D+D+h*DH+d]); float p=__expf(a*sc-mx); l+=p; for(int d=0;d<DH;d++) acc[d]+=p*b2f(qkv[j*3*D+2*D+h*DH+d]); }
  for(int d=0;d<DH;d++) o[s*D+h*DH+d]=f2b(acc[d]/l);
}
extern "C" void kernel_launch(void* const* d_in, const int* in_sizes, int n_in, void* d_out, int out_size, void* d_ws, size_t ws_size, hipStream_t stream){
  const u16* x0=(const u16*)d_in[0]; const u16* wA=(const u16*)d_in[1]; const u16* wphi=(const u16*)d_in[2]; const u16* woA=(const u16*)d_in[3];
  const u16* wB=(const u16*)d_in[5]; const u16* woB=(const u16*)d_in[6];
  char* ws=(char*)d_ws; u16* ln=(u16*)ws; u16* qkv=(u16*)(ws+(4<<20)); u16* o=(u16*)(ws+(16<<20)); u16* xa=(u16*)(ws+(20<<20)); u16* xb=(u16*)(ws+(24<<20));
  float* phi=(float*)(ws+(32<<20)); float* kv=(float*)(ws+(64<<20));
  const u16* xin=x0; u16* outs[4]={xa,xb,xa,(u16*)d_out};
  for(int L=0;L<4;L++){
    k_ln<<<S,64,0,stream>>>(xin,ln);
    k_gemm<<<dim3(27,128),256,0,stream>>>(ln,(L&1)?wB:wA,qkv,nullptr,1728,576);
    if(!(L&1)){
      k_phi<<<2*S*H/256,256,0,stream>>>(qkv,wphi,phi);
      k_kv<<<H,704,0,stream>>>(phi,qkv,kv);
      k_oA<<<S*H/256,256,0,stream>>>(phi,kv,o);
    } else k_att<<<S*H/256,256,0,stream>>>(qkv,o);
    k_gemm<<<dim3(9,128),256,0,stream>>>(o,(L&1)?woB:woA,outs[L],xin,576,576);
    xin=outs[L];
  }
}
```

```cpp
#include <hip/hip_runtime.h>
#include <type_traits>
#include <utility>
typedef unsigned short u16;
typedef unsigned long long u64;
typedef unsigned v4u __attribute__((ext_vector_type(4)));
typedef unsigned v2u __attribute__((ext_vector_type(2)));
typedef float f32x4 __attribute__((ext_vector_type(4)));
typedef float f32x2 __attribute__((ext_vector_type(2)));
typedef short bf16x8 __attribute__((ext_vector_type(8)));
typedef short s16x4 __attribute__((ext_vector_type(4)));
typedef __bf16 bf16x2 __attribute__((ext_vector_type(2)));
#define GAS __attribute__((address_space(1)))
#define LAS __attribute__((address_space(3)))
#define RLX_AGENT __ATOMIC_RELAXED, __HIP_MEMORY_SCOPE_AGENT
#define DEV __device__ __forceinline__
#define IC(n) std::integral_constant<int, (n)>{}

constexpr int WS_EPOCH = 0;
constexpr int WS_FLAGS = 4096;
constexpr int FL_RT1 = 0, FL_RT2 = 16, FL_HOP = 32;
constexpr int WQ_BLOB = 129024, WO_BLOB = 82944;
constexpr int WS_WQ = 1 << 20;
constexpr int WS_WO = WS_WQ + 2 * 16 * WQ_BLOB;
constexpr int FT = 20480;
constexpr int FRAG_BUF = 128 * FT;
constexpr int WS_FX = 8 << 20;
constexpr int WS_FO = WS_FX + 3 * FRAG_BUF;
constexpr int KVX_REC = 2304;
constexpr int KVX_BUF = 128 * 16 * KVX_REC;
constexpr int WS_KVX = WS_FO + 4 * FRAG_BUF;
constexpr int KVP_REC = 5760;
constexpr int WS_KVP = WS_KVX + 2 * KVX_BUF;
constexpr int WS_CSP = WS_KVP + 2 * 256 * KVP_REC;
constexpr int WS_DBG = WS_CSP + 2 * 16 * 18 * 448;
constexpr int WS_TOTAL = WS_DBG + 256 * 64 * 8 * 10;
static_assert(WS_WO + 2 * 8 * WO_BLOB <= WS_FX, "ws map");
constexpr int L_O = 77824, L_TOTAL = 163840;
constexpr int O_LATE = 50;
constexpr int L_STG = 8192;
constexpr int L_XCH = 31 * 1024;
constexpr int IMG_REC = 2320, STG_REC = 3472;
constexpr int L_CS = L_O + WO_BLOB;
constexpr int L_PH = L_CS + 896;
constexpr int L_ONEQ = L_TOTAL - 16;
static_assert(L_PH + 1480 <= L_ONEQ && L_PH % 8 == 0 && L_O + O_LATE * 1024 == WQ_BLOB && 24 * IMG_REC <= 55 * 1024, "lds map");
constexpr int OOB = 0x7ffffff0;
constexpr unsigned SPIN_CAP = 1u << 14;
constexpr float LOG2E = 1.4426950408889634f;
constexpr float BIG = 18446744073709551616.0f;
constexpr int RING = 14;

DEV f32x4 mfma(v4u a, v4u b, f32x4 c) {
  return __builtin_amdgcn_mfma_f32_16x16x32_bf16(__builtin_bit_cast(bf16x8, a), __builtin_bit_cast(bf16x8, b), c, 0, 0, 0);
}
DEV unsigned pk2(float lo, float hi) { f32x2 v = {lo, hi}; return __builtin_bit_cast(unsigned, __builtin_convertvector(v, bf16x2)); }
DEV v2u pk4(f32x4 v) { return v2u{pk2(v.x, v.y), pk2(v.z, v.w)}; }
DEV unsigned fu(float f) { return __builtin_bit_cast(unsigned, f); }
DEV float uf(unsigned u) { return __builtin_bit_cast(float, u); }
DEV f32x4 unpk4(v2u u) { return f32x4{uf(u.x << 16), uf(u.x & 0xffff0000u), uf(u.y << 16), uf(u.y & 0xffff0000u)}; }
DEV float dot2(unsigned a, float c) { return __builtin_amdgcn_fdot2_f32_bf16(__builtin_bit_cast(bf16x2, a), __builtin_bit_cast(bf16x2, a), c, false); }
DEV v4u cat(v2u a, v2u b) { return v4u{a.x, a.y, b.x, b.y}; }
DEV float red_add(float x) { auto a = __builtin_amdgcn_permlane16_swap(fu(x), fu(x), false, false); x = uf(a[0]) + uf(a[1]); auto b = __builtin_amdgcn_permlane32_swap(fu(x), fu(x), false, false); return uf(b[0]) + uf(b[1]); }
DEV float red_max(float x) { auto a = __builtin_amdgcn_permlane16_swap(fu(x), fu(x), false, false); x = fmaxf(uf(a[0]), uf(a[1])); auto b = __builtin_amdgcn_permlane32_swap(fu(x), fu(x), false, false); return fmaxf(uf(b[0]), uf(b[1])); }
DEV float red_min(float x) { auto a = __builtin_amdgcn_permlane16_swap(fu(x), fu(x), false, false); x = fminf(uf(a[0]), uf(a[1])); auto b = __builtin_amdgcn_permlane32_swap(fu(x), fu(x), false, false); return fminf(uf(b[0]), uf(b[1])); }
DEV float odd_rows(float x) { auto a = __builtin_amdgcn_permlane16_swap(fu(x), fu(x), false, false); return uf(a[1]); }
DEV float hi_half(float x) { auto a = __builtin_amdgcn_permlane32_swap(fu(x), fu(x), false, false); return uf(a[1]); }

typedef LAS unsigned char* lptr;
DEV v4u lds128(lptr L, int off) { return *(LAS v4u*)(L + off); }
DEV v2u lds64(lptr L, int off) { return *(LAS v2u*)(L + off); }
DEV v2u ldstr(lptr L, int off) { return __builtin_bit_cast(v2u, __builtin_amdgcn_ds_read_tr16_b64_v4i16((LAS s16x4*)(L + off))); }
DEV void sts128(lptr L, int off, v4u v) { *(LAS v4u*)(L + off) = v; }
DEV void sts64(lptr L, int off, v2u v) { *(LAS v2u*)(L + off) = v; }
#define BAR() do { asm volatile("s_waitcnt lgkmcnt(0)" ::: "memory"); __builtin_amdgcn_s_barrier(); asm volatile("" ::: "memory"); } while (0)
#define RAWBAR() do { asm volatile("" ::: "memory"); __builtin_amdgcn_s_barrier(); asm volatile("" ::: "memory"); } while (0)
#define VMWAIT(n) asm volatile("s_waitcnt vmcnt(%0)" :: "n"(n) : "memory")
#define DRAIN() VMWAIT(0)
#define PIN() __builtin_amdgcn_sched_barrier(0)
#define PINM() __builtin_amdgcn_sched_barrier(0x406)
typedef __amdgpu_buffer_rsrc_t rsrc_t;
DEV v4u gld128(rsrc_t r, int off, int soff = 0) { return __builtin_amdgcn_raw_buffer_load_b128(r, off, soff, 16); }
DEV void gst128(rsrc_t r, int off, v4u v) { __builtin_amdgcn_raw_buffer_store_b128(v, r, off, 0, 16); }
DEV void gst64(rsrc_t r, int off, v2u v) { __builtin_amdgcn_raw_buffer_store_b64(v, r, off, 0, 16); }
template <int OFF> DEV void aload(v4u& d, const unsigned char* sbase, int voff) { asm volatile("global_load_dwordx4 %0, %1, %2 offset:%3 sc1" : "=v"(d) : "v"(voff), "s"(sbase), "n"(OFF) : "memory"); }
DEV void aload64(v2u& d, const unsigned char* sbase, int voff) { asm volatile("global_load_dwordx2 %0, %1, %2 sc1" : "=v"(d) : "v"(voff), "s"(sbase) : "memory"); }
DEV void aload64p(v2u& d, const void* sbase, int voff) { asm volatile("global_load_dwordx2 %0, %1, %2" : "=v"(d) : "v"(voff), "s"(sbase) : "memory"); }
#define GOT(n, reg) asm volatile("s_waitcnt vmcnt(%1)" : "+v"(reg) : "n"(n) : "memory")
DEV void dmaf(const unsigned char* ws, int blob, unsigned ldsb, int f, int l16) {
  unsigned keep, st; int vt;
  asm volatile("s_mov_b32 %0, m0\n\ts_lshl_b32 %1, %5, 10\n\ts_add_u32 m0, %1, %4\n\ts_add_u32 %1, %1, %7\n\tv_add_u32 %2, %1, %6\n\tglobal_load_lds_dwordx4 %2, %3 sc1\n\ts_mov_b32 m0, %0"
               : "=&s"(keep), "=&s"(st), "=&v"(vt) : "s"(ws), "s"(ldsb), "s"(f), "v"(l16), "s"(__builtin_amdgcn_readfirstlane(blob)) : "memory", "scc");
}
DEV void dma_in(const void* base, int voff, unsigned ldsdst) {
  unsigned keep;
  asm volatile("s_mov_b32 %0, m0\n\ts_mov_b32 m0, %3\n\ts_nop 0\n\tglobal_load_lds_dwordx4 %1, %2\n\ts_mov_b32 m0, %0" : "=&s"(keep) : "v"(voff), "s"(base), "s"(ldsdst) : "memory");
}
template <int NW> DEV void dma_range(const unsigned char* ws, int blob, unsigned ldsbase, int f0, int f1, int w, int l16) {
  if (NW == 7 && w == 0) return;
#pragma unroll 1
  for (int f = f0 + (NW == 7 ? w - 1 : w); f < f1; f += NW) dmaf(ws, blob, ldsbase, f, l16);
}
DEV int poll_off(int lineA, int lineB, int lane) { return WS_FLAGS + ((lane & 16) ? lineB : lineA) * 128 + (lane & 15) * 8; }
DEV void poll(const unsigned char* ws, int off, bool care, u64 tag) {
  for (unsigned spins = 0; spins < SPIN_CAP; ++spins) if (__all(!care || __hip_atomic_load((const GAS u64*)(ws + off), RLX_AGENT) == tag)) break;
  __builtin_amdgcn_fence(__ATOMIC_ACQUIRE, "wavefront");
}
DEV void set_flag(unsigned char* ws, int line, int idx, u64 tag) { __hip_atomic_store((GAS u64*)(ws + WS_FLAGS + line * 128) + idx, tag, RLX_AGENT); }
DEV int unit_row(int s, int i) {
  return s < 16 ? 36 * s + (i < 16 ? i : i + 4) : 36 * (8 * (s - 16) + 2 * ((i >> 2) & 3) + (i >> 4)) + 16 + (i & 3);
}
#define TS(i) do {} while (0)

template <bool SAFE> DEV bool phi_map(f32x4 lo, f32x4 hi, v2u& pl, v2u& ml, v2u& hq) {
  float mx, mn;
  if (SAFE) {
    mx = red_max(fmaxf(fmaxf(fmaxf(lo.x, lo.y), fmaxf(lo.z, lo.w)), fmaxf(hi.x, hi.y))) * LOG2E;
    mn = red_min(fminf(fminf(fminf(lo.x, lo.y), fminf(lo.z, lo.w)), fminf(hi.x, hi.y))) * LOG2E;
  } else mx = mn = hi.x * LOG2E;
  f32x4 ep, em;
#pragma unroll
  for (int r = 0; r < 4; ++r) { ep[r] = __builtin_amdgcn_exp2f(__builtin_fmaf(lo[r], LOG2E, -mx)); em[r] = __builtin_amdgcn_exp2f(-__builtin_fmaf(lo[r], LOG2E, -mn)); }
  const float hp0 = __builtin_amdgcn_exp2f(__builtin_fmaf(hi.x, LOG2E, -mx)), hp1 = __builtin_amdgcn_exp2f(__builtin_fmaf(hi.y, LOG2E, -mx));
  const float hm0 = __builtin_amdgcn_exp2f(-__builtin_fmaf(hi.x, LOG2E, -mn)), hm1 = __builtin_amdgcn_exp2f(-__builtin_fmaf(hi.y, LOG2E, -mn));
  const float sp = red_add((ep.x + ep.y) + (ep.z + ep.w)) + (hp0 + hp1);
  const float sm = red_add((em.x + em.y) + (em.z + em.w)) + (hm0 + hm1);
  const float rp = __builtin_amdgcn_rcpf(sp), rm = __builtin_amdgcn_rcpf(sm);
  pl = pk4(ep * rp); ml = pk4(em * rm);
  hq = v2u{pk2(hp0 * rp, hp1 * rp), pk2(hm0 * rm, hm1 * rm)};
  return sp < BIG && sm < BIG;
}
DEV void phi_pair(const f32x4 (&pv)[2][2], v2u (&pl)[2], v2u (&ml)[2], v2u (&hq)[2]) {
  const bool ok0 = phi_map<false>(pv[0][0], pv[0][1], pl[0], ml[0], hq[0]);
  const bool ok1 = phi_map<false>(pv[1][0], pv[1][1], pl[1], ml[1], hq[1]);
  if (__builtin_expect(!__all(ok0 && ok1), 0)) {
    phi_map<true>(pv[0][0], pv[0][1], pl[0], ml[0], hq[0]);
    phi_map<true>(pv[1][0], pv[1][1], pl[1], ml[1], hq[1]);
  }
}
constexpr int fr_s(int ty, int i) { return i >= 90 ? (i - 90) / 2 : ty == 1 ? i / 5 : i < 54 ? i / 3 : (i - 54) / 2; }
constexpr int fr_t(int ty, int i) { return i >= 90 ? 5 + (i - 90) % 2 : ty == 1 ? i % 5 : i < 54 ? (i % 3 == 2 ? 4 : i % 3) : 2 + (i - 54) % 2; }
__host__ __device__ constexpr int fr_slot(int i) { return i < 21 ? 55 + i : i < 76 ? i - 21 : i; }
constexpr int xn(int s) { return s < 0 ? 6 : s < 13 ? 7 + s + s / 2 : 25; }
constexpr int on(int u) { return u < 0 ? 6 : u < 7 ? 7 + u : 14; }
template <int lo, int hi, class F> DEV void steps(F&& f) {
  [&]<int... k>(std::integer_sequence<int, k...>) { (f(IC(lo + k)), ...); }(std::make_integer_sequence<int, hi - lo>{});
}

__global__ void __launch_bounds__(512, 2) fused_kernel(const u16* __restrict__ x0, const u16* __restrict__ wA, const u16* __restrict__ wphi, const u16* __restrict__ woA,
                                                       const u16* __restrict__ wB, const u16* __restrict__ woB, u16* __restrict__ out, unsigned char* ws0) {
  unsigned char* const ws = ws0;
  extern __shared__ __attribute__((aligned(16))) unsigned char smem[];
  const lptr L = (lptr)smem;
  const unsigned Lb = (unsigned)(uintptr_t)L;
  const int b = blockIdx.x, tid = threadIdx.x, lane0 = tid & 63, w0 = __builtin_amdgcn_readfirstlane(tid >> 6);
  const int p0 = ((b & 7) >> 1) * 4 + (b >> 6), g0 = (b & 1) * 8 + ((b >> 3) & 7);
  const rsrc_t rs = __builtin_amdgcn_make_buffer_rsrc((void*)ws, 0, WS_TOTAL, 0x00020000);
  TS(0);
  u64 tag;
  v4u xf[18];
  f32x4 res[3];
  v4u ring[15];
  float ssq;
  const f32x4 z = {0.f, 0.f, 0.f, 0.f};
  auto colsums7 = [&](int type) {
    int lane = lane0;
    asm volatile("" : "+v"(lane));
    const int base = WS_CSP + (type * 16 + g0) * 18 * 448 + (lane >> 5) * 9 * 448 + (lane & 31) * 16;
    v4u c[9];
#pragma unroll
    for (int i = 0; i < 9; ++i) c[i] = gld128(rs, (lane & 31) < 28 ? base + i * 448 : OOB);
    f32x4 t = __builtin_bit_cast(f32x4, c[0]);
#pragma unroll
    for (int i = 1; i < 9; ++i) t += __builtin_bit_cast(f32x4, c[i]);
#pragma unroll
    for (int r = 0; r < 4; ++r) { auto sw = __builtin_amdgcn_permlane32_swap(fu(t[r]), fu(t[r]), false, false); t[r] = uf(sw[0]) + uf(sw[1]); }
    if (lane < 28) *(LAS f32x4*)(L + L_CS + type * 448 + lane * 16) = t;
  };
  {
    const int lane = lane0, tok = lane & 15, gq = lane >> 4, w = w0, p = p0, g = g0, T = 8 * p + w;
    const int trq = (lane >> 2) & 3, trp = lane & 3;
    constexpr int RAW_A = 147456;
    constexpr int RAW_B = WQ_BLOB, RAW_OA = RAW_B + 6912, RAW_OB = RAW_OA + 2304, RAW_E1 = RAW_OB + 2304;
    const v4u ones = {0x3F803F80u, 0x3F803F80u, 0x3F803F80u, 0x3F803F80u};
    const int pc0 = tid, pc1 = tid + 512 > 863 ? 863 : tid + 512;
    const int i0 = pc0 / 27, q0 = pc0 - i0 * 27, i1 = pc1 / 27, q1 = pc1 - i1 * 27;
    const int co0 = (q0 / 9) * 1080 + q0 * 8 + g * 72, co1 = (q1 / 9) * 1080 + q1 * 8 + g * 72;
    auto wq_load = [&](const u16* W, int s, v2u (&d)[2]) { aload64p(d[0], W, unit_row(s, i0) * 3456 + co0); aload64p(d[1], W, unit_row(s, i1) * 3456 + co1); };
    auto wq_put = [&](int img, const v2u (&d)[2]) { sts64(L, img + pc0 * 8, d[0]); sts64(L, img + pc1 * 8, d[1]); };
    const int wco = (w & 1) * 40 + (w < 2 ? 72 : w < 4 ? 144 : 0) - (w > 4 ? 40 : 0) + (w == 6 ? 80 : 0);
    auto wq_emit = [&](int img, int type, int s) {
      if (w < 7) {
        int a0 = img + (4 * gq + trq) * 216 + (w == 4 ? trp * 72 + 32 : wco + trp * 8), a1 = a0 + 16 * 216;
        if (w == 4 && trp == 3) a0 = a1 = L_ONEQ;
        const int j = w >= 5 ? 90 + 2 * s + w - 5 : type == 1 ? 5 * s + w : (w == 2 || w == 3) ? 54 + 2 * s + w - 2 : 3 * s + (w >> 1 ? 2 : w);
        const v4u f = cat(ldstr(L, a0), ldstr(L, a1));
        gst128(rs, WS_WQ + (type * 16 + g) * WQ_BLOB + fr_slot(j) * 1024 + lane * 16, f);
        gst128(rs, tok == 0 ? WS_CSP + ((type * 16 + g) * 18 + s) * 448 + w * 64 + gq * 16 : OOB, __builtin_bit_cast(v4u, mfma(f, ones, z)));
      }
    };
    const int po = tid > 287 ? 287 : tid, io = po / 9;
    auto wo_load = [&](const u16* W, int s, v2u& d) { aload64p(d, W, unit_row(s, io) * 1152 + g * 72 + (po - io * 9) * 8); };
    auto wo_emit = [&](int img, int type, int s, int t) {
      const int a0 = img + (4 * gq + trq) * 72 + (t == 2 ? 32 : t * 40 + trp * 8);
      const int idx = s < 16 ? 2 * (s & 7) + 2 + (s >> 3) : s - 16;
      const int o = WS_WO + (type * 8 + (g >> 1)) * WO_BLOB + (17 - idx) * 4608 + (t < 2 ? (g & 1) * 2048 + t * 1024 + lane * 16 : 4096 + (gq * 8 + (g & 1) * 4 + tok) * 16);
      gst128(rs, (t == 2 && tok >= 4) ? OOB : o, cat(ldstr(L, a0), ldstr(L, a0 + 16 * 72)));
    };
    v2u dA[2], dE[2], dB[2], dO[3], r0, r1, r2, ep, dP;
    const int xs = 16 + (p & 1);
    wq_load(wA, p, dA);
    wq_load(p < 2 ? wA : wB, xs, dE);
    aload64(ep, ws, WS_EPOCH);
    PIN();
    auto x_dma = [&](auto i_) {
      constexpr int i = decltype(i_)::value;
      const int ci = i * 64 + lane, r = (ci * 911) >> 16;
      const int src = T * 18432 + ci * 16 - r * 16 + (ci - r * 72 < r ? 1152 : 0);
      dma_in(x0, src, Lb + w * 18432 + i * 1024);
    };
    steps<0, 6>(x_dma);
    if (tid == 0) sts64(L, L_ONEQ, v2u{0x3F80u, 0u});
    asm volatile("s_waitcnt vmcnt(6)" : "+v"(dA[0]), "+v"(dA[1]), "+v"(dE[0]), "+v"(dE[1]), "+v"(ep) :: "memory");
    wq_put(RAW_A, dA);
    tag = ((u64)(unsigned)__builtin_amdgcn_readfirstlane(ep.x) | ((u64)(unsigned)__builtin_amdgcn_readfirstlane(ep.y) << 32)) + 1;
    BAR();
    wq_emit(RAW_A, 0, p);
    if (p < 2) { BAR(); wq_put(RAW_A, dE); BAR(); wq_emit(RAW_A, 0, xs); }
    PIN();
    steps<6, 12>(x_dma);
    VMWAIT(6);
    RAWBAR();
    if (tid == 448) set_flag(ws, FL_RT1 + g, p, tag);
    TS(1);
    steps<12, 18>(x_dma);
    wq_load(wB, p, dB); wo_load(woA, p, dO[0]); wo_load(woB, p, dO[1]); wo_load(p < 6 ? woA : woB, xs, dO[2]);
    const int xr = (16 * (8 * p + 4 * (g & 1) + (w & 3)) + tok) * 1152 + 72 * ((g & ~1) + (w >> 2));
    aload64p(r0, x0, xr + 8 * gq); aload64p(r1, x0, xr + 40 + 8 * gq); aload64p(r2, x0, xr + 32);
    const int phi_i = tid < 162 ? tid : 161;
    aload64p(dP, wphi, g * 1296 + phi_i * 8);
    VMWAIT(9);
    {
      const int xb = w * 18432 + tok * 1152;
      auto xoff = [&](int byte) { int c = (byte >> 4) + tok; c -= c >= 72 ? 72 : 0; return xb + c * 16 + (byte & 8); };
#pragma unroll
      for (int s = 0; s < 16; ++s) xf[s] = cat(lds64(L, xoff(72 * s + 8 * gq)), lds64(L, xoff(72 * s + 40 + 8 * gq)));
#pragma unroll
      for (int s = 0; s < 2; ++s) xf[16 + s] = cat(lds64(L, xoff(72 * (8 * s + 2 * gq) + 32)), lds64(L, xoff(72 * (8 * s + 2 * gq + 1) + 32)));
    }
    TS(2);
    if (w == 0) poll(ws, poll_off(FL_RT1 + g, FL_RT1 + g, lane), true, tag);
    asm volatile("s_waitcnt vmcnt(0)" : "+v"(dB[0]), "+v"(dB[1]), "+v"(dO[0]), "+v"(dO[1]), "+v"(dO[2]), "+v"(r0), "+v"(r1), "+v"(r2), "+v"(dP) :: "memory");
    BAR();
    TS(3);
    if (w == 0) colsums7(0);
    dma_range<7>(ws, WS_WQ + g * WQ_BLOB, Lb, 0, 126, w, lane * 16);
    wq_put(RAW_B, dB); sts64(L, RAW_OA + po * 8, dO[0]); sts64(L, RAW_OB + po * 8, dO[1]);
    if (p >= 2 && p < 4) wq_put(RAW_E1, dE);
    else if (p >= 4 && p < 8) sts64(L, RAW_E1 + po * 8, dO[2]);
    { const int d0 = 2 * phi_i, d1 = d0 + 1; *(LAS unsigned*)(L + L_PH + (d0 / 9) * 4 + d0 * 4) = dP.x; *(LAS unsigned*)(L + L_PH + (d1 / 9) * 4 + d1 * 4) = dP.y; }
    if (tid >= 448 && tid < 448 + 46) { const int i = tid - 448; *(LAS unsigned*)(L + L_PH + (i < 36 ? i * 40 + 36 : 1440 + (i - 36) * 4)) = 0u; }
    res[0] = unpk4(r0); res[1] = unpk4(r1); res[2] = unpk4(r2);
    {
      float q4[4] = {0.f, 0.f, 0.f, 0.f};
#pragma unroll
      for (int s = 0; s < 18; ++s)
#pragma unroll
        for (int j = 0; j < 4; ++j) q4[j] = dot2(xf[s][j], q4[j]);
      ssq = (q4[0] + q4[1]) + (q4[2] + q4[3]);
    }
    DRAIN();
    BAR();
    TS(4);
    wq_emit(RAW_B, 1, p);
    if (p >= 2 && p < 4) wq_emit(RAW_E1, 1, xs);
    if (w < 3) wo_emit(RAW_OA, 0, p, w); else if (w < 6) wo_emit(RAW_OB, 1, p, w - 3);
    if (p >= 4 && p < 8 && w < 3) wo_emit(RAW_E1, p < 6 ? 0 : 1, xs, w);
#pragma unroll
    for (int i = 0; i < RING; ++i) ring[i] = lds128(L, fr_slot(i) * 1024 + lane * 16);
    PIN();
  }

  auto layer = [&](auto ty_, const int it) __attribute__((always_inline)) {
    constexpr int ty = decltype(ty_)::value;
    const int Lr = 2 * it + ty;
    int lane = lane0, w = w0, p = p0, g = g0;
    unsigned char* ws = ws0;
    asm volatile("" : "+v"(lane), "+s"(w), "+s"(p), "+s"(g), "+s"(ws));
    const int T = 8 * p + w;
    const int tok = lane & 15, gq = lane >> 4, trq = (lane >> 2) & 3, trp = lane & 3, l16 = lane * 16;
    const bool last = (ty == 1) && (it == 1);
    int hi16 = l16 + 65536;
    asm volatile("" : "+v"(hi16));
    auto wfrag = [&](int slot) { return slot < 64 ? lds128(L, l16 + slot * 1024) : lds128(L, hi16 + (slot - 64) * 1024); };
    const int curW = WS_WQ + (ty * 16 + g) * WQ_BLOB, nxtW = WS_WQ + ((1 - ty) * 16 + g) * WQ_BLOB, curO = WS_WO + (ty * 8 + (g >> 1)) * WO_BLOB;
    const int flm = FL_HOP + Lr * 48;
    v4u rv[4];
    const int xtb = WS_FX + (Lr - 1) * FRAG_BUF + T * FT, xv0 = xtb + 4096 + l16, xv1 = xv0 + 8192, xvr = xtb + 16384 + gq * 512 + tok * 16;
    auto x_op = [&](auto k_) {
      constexpr int k = decltype(k_)::value;
      if constexpr (k < 16) aload<(k % 8) * 1024 - 4096>(xf[k], ws, k < 8 ? xv0 : xv1);
      else if constexpr (k < 20) aload<((k - 16) & 1) * 256 + ((k - 16) >> 1) * 2048>(rv[k - 16], ws, xvr);
      else dmaf(ws, curW, Lb, min(90 + (k - 20) * 8 + w, 125), l16);
    };
    if (Lr > 0) steps<0, xn(-1)>(x_op);
    PIN();
    f32x4 acc[7];
#pragma unroll
    for (int t = 0; t < 7; ++t) acc[t] = z;
    constexpr int QB = ty == 0 ? 72 : 88;
    auto qkv_step = [&](auto i_) {
      constexpr int i = decltype(i_)::value, s = fr_s(ty, i), t = fr_t(ty, i), P1 = ty == 0 ? 3 : 5;
      if (i < 16 * P1 && t == 0) { PIN(); if (Lr > 0) steps<xn(s - 1), xn(s)>(x_op); PIN(); GOT(xn(s) - s - 1, xf[s]); }
      if (i == 16 * P1) {
        PIN();
        asm volatile("s_waitcnt vmcnt(5)" : "+v"(rv[0]), "+v"(rv[1]), "+v"(rv[2]), "+v"(rv[3]) :: "memory");
        if (Lr > 0) { xf[16] = v4u{rv[0].x, rv[0].y, rv[1].x, rv[1].y}; xf[17] = v4u{rv[2].x, rv[2].y, rv[3].x, rv[3].y}; ssq = (uf(rv[0].z) + uf(rv[1].z)) + (uf(rv[2].z) + uf(rv[3].z)); }
      }
      if (i == QB) {
        DRAIN(); RAWBAR();
#pragma unroll
        for (int j = 90; j < QB + RING; ++j) ring[j % RING] = wfrag(fr_slot(j));
      }
      acc[t] = mfma(ring[i % RING], xf[s], acc[t]);
      if (i + RING < 126 && (i + RING < 90 || i >= QB)) ring[i % RING] = wfrag(fr_slot(i + RING));
      if (ty == 0 && i >= 54 && i < 90) {
        __builtin_amdgcn_sched_group_barrier(0x008, 1, 0); __builtin_amdgcn_sched_group_barrier(0x100, 1, 0);
        __builtin_amdgcn_sched_group_barrier(0x002, 4, 0);
      } else PINM();
    };
    const int csb = L_CS + ty * 448 + gq * 16;
    auto wph = [&](int h, int ft) {
      const int rl = L_PH + (20 * h + 4 * gq + trq) * 40, ru = L_PH + ((trq >> 1) == h ? (18 * h + (h ? 0 : 16) + (trq & 1)) * 40 : 1440);
      return cat(ldstr(L, rl + (ft ? 32 : trp * 8)), ldstr(L, ru + (ft ? 32 : trp * 8)));
    };
    const int vsw = gq * 128 + ((tok * 8) ^ ((gq >> 1) * 64));
    const int trl = trp * 128 + (((4 * gq + trq) * 8) ^ ((trp >> 1) * 64));
    steps<0, ty == 0 ? 54 : 90>(qkv_step);
    if (Lr == 0) {
      DRAIN();
      RAWBAR();
      if (tid == 448) set_flag(ws, FL_RT2 + g, p, tag);
    }
    const float mean = hi_half(odd_rows(acc[4].x)) * (1.0f / 576.0f);
    const float var = red_add(ssq) * (1.0f / 576.0f) - mean * mean;
    const float rstd = __builtin_amdgcn_rsqf(fmaxf(var, 0.f) + 1e-5f);
    auto fix = [&](int t, float sc) { return (acc[t] - mean * *(LAS f32x4*)(L + csb + t * 64)) * sc; };
    v2u kq[2], vq[2];
    kq[0] = pk4(fix(0, rstd)); kq[1] = pk4(fix(1, rstd));
    const f32x4 remf = fix(4, rstd);
    const v2u remq = pk4(remf);
    const int kh = w >= 3 ? 1 : 0, kft = w - 3 * kh;
    if (ty == 0) {
      const v2u ku = gq == 1 ? remq : v2u{0u, 0u};
      f32x4 pv[2][2];
#pragma unroll
      for (int h = 0; h < 2; ++h) { const v4u bk = cat(kq[h], ku); pv[h][0] = mfma(wph(h, 0), bk, z); pv[h][1] = mfma(wph(h, 1), bk, z); }
      PIN();
      v2u pl[2], ml[2], hq[2];
      const bool ok0 = phi_map<false>(pv[0][0], pv[0][1], pl[0], ml[0], hq[0]);
      steps<54, 72>(qkv_step);
      PIN();
      const bool ok1 = phi_map<false>(pv[1][0], pv[1][1], pl[1], ml[1], hq[1]);
      steps<72, 90>(qkv_step);
      PIN();
      if (__builtin_expect(!__all(ok0 && ok1), 0)) {
        phi_map<true>(pv[0][0], pv[0][1], pl[0], ml[0], hq[0]);
        phi_map<true>(pv[1][0], pv[1][1], pl[1], ml[1], hq[1]);
      }
      vq[0] = pk4(fix(2, rstd)); vq[1] = pk4(fix(3, rstd));
      TS(8 + Lr * 12 + 0);
      RAWBAR();
      const int sb = L_STG + w * STG_REC;
      sts64(L, sb + vsw, vq[0]); sts64(L, sb + 512 + vsw, vq[1]);
      if (gq == 2) sts64(L, sb + 3072 + tok * 8, remq);
      if (lane >= 62) sts64(L, sb + 3456 + (lane & 1) * 8, v2u{(lane & 1) ? 0u : 0x3F80u, 0u});
#pragma unroll
      for (int h = 0; h < 2; ++h) {
        sts64(L, sb + 1024 + h * 1024 + vsw, pl[h]); sts64(L, sb + 1536 + h * 1024 + vsw, ml[h]);
        if (gq == 0) sts64(L, sb + 3200 + h * 128 + tok * 8, hq[h]);
      }
      BAR();
      if (w < 6) {
        const int aa = L_STG + (kft < 2 ? 1024 + kh * 1024 + kft * 512 + trl : (trp == 0 ? 3200 + kh * 128 + (4 * gq + trq) * 8 : 3464));
        const int bl = L_STG + kh * 512 + trl, bh = L_STG + (trp == 0 ? 3072 + (4 * gq + trq) * 8 : 3456);
        v4u fa[4], fl[4], fh[4];
#pragma unroll
        for (int ks = 0; ks < 4; ++ks) {
          const int o0 = 2 * ks * STG_REC, o1 = o0 + STG_REC;
          fa[ks] = cat(ldstr(L, aa + o0), ldstr(L, aa + o1)); fl[ks] = cat(ldstr(L, bl + o0), ldstr(L, bl + o1)); fh[ks] = cat(ldstr(L, bh + o0), ldstr(L, bh + o1));
        }
        PIN();
        f32x4 dl = z, dh = z;
#pragma unroll
        for (int ks = 0; ks < 4; ++ks) { dl = mfma(fa[ks], fl[ks], dl); dh = mfma(fa[ks], fh[ks], dh); }
        const int rb = WS_KVP + ((it * 16 + g) * 16 + p) * KVP_REC + kh * 2880;
        const int slot = tok == 4 ? 2 : (tok >> 1) == kh ? (tok & 1) : -1;
        const bool rowok = kft < 2 || gq == 0;
        if (rowok) gst128(rs, rb + kft * 1024 + l16, __builtin_bit_cast(v4u, dl));
        if (rowok && slot >= 0) gst128(rs, rb + 2304 + kft * 256 + (gq * 4 + slot) * 16, __builtin_bit_cast(v4u, dh));
      } else {
#pragma unroll
        for (int j = 0; j < 4; ++j) { unsigned zz = 0u; asm volatile("" : "+v"(zz)); sts128(L, (w - 6) * 4096 + j * 1024 + l16, v4u{zz, zz, zz, zz}); }
        if (it == 0 && w == 6) { poll(ws, poll_off(FL_RT2 + g, FL_RT2 + (g ^ 1), lane), true, tag); colsums7(1); }
      }
    } else {
      const int rb = WS_KVX + it * KVX_BUF + (T * 16 + g) * KVX_REC;
      gst128(rs, rb + l16, cat(kq[0], kq[1]));
      if (gq == 1 || gq == 2) gst64(rs, rb + 2048 + (gq - 1) * 128 + tok * 8, remq);
      vq[0] = pk4(fix(2, rstd)); vq[1] = pk4(fix(3, rstd));
      TS(8 + Lr * 12 + 0);
      gst128(rs, rb + 1024 + l16, cat(vq[0], vq[1]));
    }
    PIN();
    steps<90, 108>(qkv_step);
    DRAIN();
    RAWBAR();
    if (tid == 448) set_flag(ws, flm + g, p, tag);
    TS(8 + Lr * 12 + 1);
    if (ty == 1) {
      const int ib = (16 + w) * IMG_REC;
      sts64(L, ib + lane * 8, kq[0]); sts64(L, ib + 512 + lane * 8, kq[1]);
      sts64(L, ib + 1024 + vsw, vq[0]); sts64(L, ib + 1536 + vsw, vq[1]);
      if (gq == 1 || gq == 2) sts64(L, ib + 2048 + (gq - 1) * 128 + tok * 8, remq);
      if (lane < 3) { const int i = w + 8 * lane; sts128(L, i * IMG_REC + 2304, v4u{0x3F80u, 0u, (8 * (p - 2) + i >= 0) ? 0u : 0xF149u, 0u}); }
    } else dma_range<7>(ws, nxtW, Lb, 55, 76, w, l16);
    dma_range<7>(ws, curO, Lb + L_O, O_LATE, 81, w, l16);
    PIN();
    steps<108, 126>(qkv_step);
    constexpr float QS = 0.23570226039551584f * LOG2E;
    const v2u q0 = pk4(fix(5, ty == 1 ? rstd * QS : rstd)), q1 = pk4(fix(6, ty == 1 ? rstd * QS : rstd));
    v2u olo[2]; unsigned ohi[2];
    if (ty == 0) {
      TS(8 + Lr * 12 + 2);
      if (w == 0) poll(ws, poll_off(flm + g, flm + g, lane), true, tag);
      RAWBAR();
      TS(8 + Lr * 12 + 3);
      const int pi = w * 64 + lane;
      v4u dp[16];
      if (w < 6) {
        const int po = pi < 360 ? WS_KVP + (it * 16 + g) * 16 * KVP_REC + pi * 16 : OOB;
#pragma unroll
        for (int q = 0; q < 16; ++q) dp[q] = gld128(rs, po, q * KVP_REC);
      }
      PIN();
      v4u qf[2][2];
      {
        const v2u qu = gq == 0 ? remq : v2u{0u, 0u};
        f32x4 pv[2][2];
#pragma unroll
        for (int h = 0; h < 2; ++h) { const v4u bq = cat(h ? q1 : q0, qu); pv[h][0] = mfma(wph(h, 0), bq, z); pv[h][1] = mfma(wph(h, 1), bq, z); }
        v2u pl[2], ml[2], hq[2];
        phi_pair(pv, pl, ml, hq);
#pragma unroll
        for (int h = 0; h < 2; ++h) { qf[h][0] = cat(pl[h], ml[h]); qf[h][1] = cat(hq[h], v2u{0u, 0u}); }
      }
      PIN();
      if (w < 6) {
        f32x4 s0 = __builtin_bit_cast(f32x4, dp[0]), s1 = __builtin_bit_cast(f32x4, dp[1]);
#pragma unroll
        for (int q = 2; q < 16; q += 2) { s0 += __builtin_bit_cast(f32x4, dp[q]); s1 += __builtin_bit_cast(f32x4, dp[q + 1]); }
        const v2u sv = pk4(s0 + s1);
        const int h = pi >= 180 ? 1 : 0, j = pi - 180 * h;
        const int e = j - 144, sl = e & 3, half = j < 144 ? (j >> 6) & 1 : (e >> 4) & 1;
        const int blk = j < 128 ? 0 : j < 144 ? 1024 : e < 32 ? 2048 : 3072;
        const int ln = j < 144 ? (j & 63) : ((e >> 2) & 3) * 16 + (sl < 2 ? 2 * h + sl : 4);
        const int dst = h * 4096 + blk + ln * 16 + half * 8;
        if (pi < 360 && (j < 144 || sl < 3)) sts64(L, dst, sv);
        if (pi < 360 && j >= 144 && sl == 2) sts64(L, dst + 128, sv);
      }
      BAR();
      TS(8 + Lr * 12 + 4);
      v4u kf[2][4];
#pragma unroll
      for (int h = 0; h < 2; ++h)
#pragma unroll
        for (int j = 0; j < 4; ++j) kf[h][j] = lds128(L, h * 4096 + j * 1024 + l16);
      PIN();
#pragma unroll
      for (int h = 0; h < 2; ++h) {
        const f32x4 dl2 = mfma(kf[h][1], qf[h][1], mfma(kf[h][0], qf[h][0], z));
        const f32x4 dh2 = mfma(kf[h][3], qf[h][1], mfma(kf[h][2], qf[h][0], z));
        const float r = __builtin_amdgcn_rcpf(odd_rows(dh2.x) + 1e-6f);
        olo[h] = pk4(dl2 * r); ohi[h] = h == 0 ? pk2(dh2.x * r, dh2.y * r) : pk2(dh2.z * r, dh2.w * r);
      }
    } else {
      v4u qf[2];
      {
        const v2u qr = pk4(remf * QS);
        const unsigned bias1 = gq == 1 ? 0x3F80u : 0u;
        qf[0] = cat(q0, v2u{gq == 0 ? qr.x : bias1, 0u});
        qf[1] = cat(q1, v2u{bias1, gq == 0 ? qr.y : 0u});
      }
      const int wb = w * IMG_REC;
      const int kuo = wb + (gq == 1 ? 2312 : 2048 + tok * 8);
      auto kfrag = [&](int h, int i) { return cat(lds64(L, wb + h * 512 + lane * 8 + i * IMG_REC), lds64(L, kuo + i * IMG_REC)); };
      float nm[2];
#pragma unroll
      for (int h = 0; h < 2; ++h) {
        const f32x4 sd = mfma(cat(kq[h], gq == 1 ? v2u{0u, 0u} : lds64(L, kuo + 16 * IMG_REC)), qf[h], z);
        float m = (4 * gq <= tok) ? sd.x : -3e38f;
#pragma unroll
        for (int r = 1; r < 4; ++r) m = fmaxf(m, (4 * gq + r <= tok) ? sd[r] : -3e38f);
        nm[h] = -red_max(m);
      }
      PIN();
      TS(8 + Lr * 12 + 2);
      if (w == 0) poll(ws, poll_off(flm + g, flm + g, lane), tok == p - 1 || tok == p - 2, tag);
      RAWBAR();
      TS(8 + Lr * 12 + 3);
      {
        v4u kk[2], vv[2];
#pragma unroll
        for (int j = 0; j < 2; ++j) {
          const int Tg = 8 * (p - 2) + w + 8 * j;
          const int rb = Tg >= 0 ? WS_KVX + it * KVX_BUF + (Tg * 16 + g) * KVX_REC + l16 : OOB;
          kk[j] = gld128(rs, rb); vv[j] = gld128(rs, rb, 1024);
        }
        const int Tr = 8 * (p - 2) + w + 8 * (gq & 1);
        const v4u rr = gld128(rs, (Tr >= 0 && gq < 2) ? WS_KVX + it * KVX_BUF + (Tr * 16 + g) * KVX_REC + 2048 + tok * 16 : OOB);
        PIN();
#pragma unroll
        for (int j = 0; j < 2; ++j) {
          const int ib = (w + 8 * j) * IMG_REC;
          sts64(L, ib + lane * 8, v2u{kk[j].x, kk[j].y}); sts64(L, ib + 512 + lane * 8, v2u{kk[j].z, kk[j].w});
          sts64(L, ib + 1024 + vsw, v2u{vv[j].x, vv[j].y}); sts64(L, ib + 1536 + vsw, v2u{vv[j].z, vv[j].w});
        }
        if (gq < 2) sts128(L, (w + 8 * gq) * IMG_REC + 2048 + tok * 16, rr);
      }
      BAR();
      TS(8 + Lr * 12 + 4);
      const int vh = wb + (trp == 0 ? 2176 + (4 * gq + trq) * 8 : 2304);
      const unsigned m0 = (4 * gq + 0 <= tok ? 0xffffu : 0u) | (4 * gq + 1 <= tok ? 0xffff0000u : 0u);
      const unsigned m1 = (4 * gq + 2 <= tok ? 0xffffu : 0u) | (4 * gq + 3 <= tok ? 0xffff0000u : 0u);
#pragma unroll 1
      for (int pass = 0; pass < 2; ++pass) {
        bool ok = true;
#pragma unroll
        for (int h = 0; h < 2; ++h) {
          const f32x4 c = {nm[h], nm[h], nm[h], nm[h]};
          f32x4 sc[17];
          {
            v4u kf[17];
#pragma unroll
            for (int j = 0; j < 17; ++j) { const int i = j == 0 ? 16 : j - 1; kf[i] = kfrag(h, i); }
            PIN();
#pragma unroll
            for (int j = 0; j < 17; ++j) { const int i = j == 0 ? 16 : j - 1; sc[i] = mfma(kf[i], qf[h], c); }
          }
          const int vl = wb + 1024 + h * 512 + trl;
          v4u va[9], vb[9];
#pragma unroll
          for (int ks = 0; ks < 9; ++ks) {
            const int i0 = ks == 0 ? 0 : 2 * ks - 1, i1 = ks == 0 ? 16 : ks < 8 ? 2 * ks : 15;
            va[ks] = cat(ldstr(L, vl + i0 * IMG_REC), ldstr(L, vl + i1 * IMG_REC));
            vb[ks] = cat(ldstr(L, vh + i0 * IMG_REC), ldstr(L, vh + i1 * IMG_REC));
          }
          PINM();
#pragma unroll
          for (int r = 0; r < 4; ++r) sc[0][r] = (4 * gq + r <= tok) ? sc[16][r] : sc[0][r];
          auto pexp = [&](int i) {
            f32x4 e;
#pragma unroll
            for (int r = 0; r < 4; ++r) e[r] = __builtin_amdgcn_exp2f(sc[i][r]);
            return pk4(e);
          };
          f32x4 dl, dh;
          {
            const v2u p0 = pexp(0);
            const v4u bp = {p0.x & ~m0, p0.y & ~m1, p0.x & m0, p0.y & m1};
            dl = mfma(va[0], bp, z); dh = mfma(vb[0], bp, z);
          }
#pragma unroll
          for (int ks = 1; ks < 9; ++ks) {
            const v4u bp = cat(pexp(2 * ks - 1), ks < 8 ? pexp(2 * ks) : v2u{0u, 0u});
            dl = mfma(va[ks], bp, dl); dh = mfma(vb[ks], bp, dh);
            const int slot = h * 8 + ks - 1;
            int wo_ = w; asm volatile("" : "+s"(wo_));
            if (slot < 7) dmaf(ws, curO, Lb + L_O, min(slot * 8 + wo_, O_LATE - 1), l16);
            else if (slot < 10 && !last) dmaf(ws, nxtW, Lb, min(55 + (slot - 7) * 8 + wo_, 75), l16);
          }
          const float l = odd_rows(dh.x);
          ok = ok && l < BIG;
          const float r = __builtin_amdgcn_rcpf(l);
          olo[h] = pk4(dl * r); ohi[h] = h == 0 ? pk2(dh.x * r, dh.y * r) : pk2(dh.z * r, dh.w * r);
          PIN();
        }
        if (__builtin_expect(__all(ok), 1)) break;
#pragma unroll
        for (int h = 0; h < 2; ++h) {
          const f32x4 s0 = mfma(kfrag(h, 0), qf[h], z), s16 = mfma(kfrag(h, 16), qf[h], z);
          float m = -3e38f;
#pragma unroll
          for (int r = 0; r < 4; ++r) m = fmaxf(m, (4 * gq + r <= tok) ? s16[r] : s0[r]);
#pragma unroll 1
          for (int i = 1; i < 16; ++i) { const f32x4 s = mfma(kfrag(h, i), qf[h], z); m = fmaxf(fmaxf(fmaxf(m, s.x), fmaxf(s.y, s.z)), s.w); }
          nm[h] = -red_max(m);
        }
      }
    }
    TS(8 + Lr * 12 + 5);
    gst128(rs, WS_FO + Lr * FRAG_BUF + T * FT + g * 1024 + l16, cat(olo[0], olo[1]));
    gst128(rs, gq == 0 ? WS_FO + Lr * FRAG_BUF + T * FT + 16384 + g * 256 + tok * 16 : OOB, v4u{ohi[0], ohi[1], 0u, 0u});
    const int kw = w >> 2, To = 8 * p + 4 * (g & 1) + (w & 3), go = (g & ~1) + kw;
    int omine = L_O + (1 - kw) * 4608 + kw * 2048 + l16, oother = L_O + (1 - kw) * 4608 + (1 - kw) * 2048 + l16, orem = L_O + (1 - kw) * 4608 + 4096 + (gq * 8 + (tok & 7)) * 16;
    asm volatile("" : "+v"(omine), "+v"(oother), "+v"(orem));
    auto ofrag = [&](int i) { const int u = i / 5, t = i % 5; return lds128(L, (8 - u) * 9216 + (t < 2 ? omine + t * 1024 : t < 4 ? oother + (t - 2) * 1024 : orem)); };
#pragma unroll
    for (int i = 0; i < 15; ++i) ring[i] = ofrag(i);
    PIN();
    DRAIN();
    RAWBAR();
    if (tid == 448) set_flag(ws, flm + 16 + p, g, tag);
    TS(8 + Lr * 12 + 6);
    if (ty == 0) { dma_range<7>(ws, curO, Lb + L_O, 0, O_LATE, w, l16); DRAIN(); }
    if (w == 0) poll(ws, poll_off(flm + 16 + p, flm + 16 + p, lane), true, tag);
    RAWBAR();
    TS(8 + Lr * 12 + 7);
    const int ob = WS_FO + Lr * FRAG_BUF + To * FT, ov0 = ob + kw * 8192 + 4096 + l16, ovr = ob + 16384 + kw * 2048 + gq * 512 + tok * 16;
    auto o_op = [&](auto k_) {
      constexpr int k = decltype(k_)::value;
      if constexpr (k < 2) aload<k * 256>(rv[k], ws, ovr);
      else if constexpr (k < 10) aload<(k - 2) * 1024 - 4096>(xf[k - 1], ws, ov0);
      else dmaf(ws, nxtW, Lb, min((k - 10) * 8 + w, 30), l16);
    };
    steps<0, on(-1)>(o_op);
    PIN();
    f32x4 oa[5] = {res[0], res[1], z, z, z};
    auto wo_step = [&](auto i_) {
      constexpr int i = decltype(i_)::value, u = i / 5, t = i % 5;
      if (t == 0) {
        PIN(); steps<on(u - 1), on(u)>(o_op); PIN();
        if (u == 0) {
          asm volatile("s_waitcnt vmcnt(%2)" : "+v"(rv[0]), "+v"(rv[1]) : "n"(on(0) - 2) : "memory");
          xf[0] = v4u{rv[0].x, rv[0].y, rv[1].x, rv[1].y};
        } else GOT(on(u) - u - 2, xf[u]);
      }
      oa[t] = mfma(ring[i % 15], xf[u], oa[t]);
      if (i + 15 < 45) ring[i % 15] = ofrag(i + 15);
      PINM();
    };
    steps<0, 45>(wo_step);
    {
      const int xm = L_XCH + w * 3072 + l16, xo = L_XCH + (w ^ 4) * 3072 + l16;
      sts128(L, xm, __builtin_bit_cast(v4u, oa[2])); sts128(L, xm + 1024, __builtin_bit_cast(v4u, oa[3])); sts128(L, xm + 2048, __builtin_bit_cast(v4u, oa[4]));
      BAR();
      res[0] = oa[0] + __builtin_bit_cast(f32x4, lds128(L, xo)); res[1] = oa[1] + __builtin_bit_cast(f32x4, lds128(L, xo + 1024));
      f32x4 rm = oa[4] + __builtin_bit_cast(f32x4, lds128(L, xo + 2048));
#pragma unroll
      for (int r = 0; r < 4; ++r) { auto sw = __builtin_amdgcn_permlane16_swap(fu(rm[r]), fu(rm[r]), false, false); rm[r] = uf(kw ? sw[1] : sw[0]); }
      res[2] += rm;
    }
    TS(8 + Lr * 12 + 8);
    const v2u xa = pk4(res[0]), xb = pk4(res[1]), xc = pk4(res[2]);
    if (!last) {
      const float sq = red_add(dot2(xa.x, dot2(xa.y, 0.f)) + dot2(xb.x, dot2(xb.y, 0.f))) + dot2(xc.x, dot2(xc.y, 0.f));
      gst128(rs, WS_FX + Lr * FRAG_BUF + To * FT + go * 1024 + l16, cat(xa, xb));
      gst128(rs, gq == 0 ? WS_FX + Lr * FRAG_BUF + To * FT + 16384 + go * 256 + tok * 16 : OOB, v4u{xc.x, xc.y, fu(sq), 0u});
#pragma unroll
      for (int i = 0; i < RING; ++i) ring[i] = wfrag(fr_slot(i));
      PIN();
      DRAIN();
      RAWBAR();
      if (tid == 448) set_flag(ws, flm + 32 + p, g, tag);
      TS(8 + Lr * 12 + 9);
      dma_range<7>(ws, nxtW, Lb, 31, 55, w, l16);
      dma_range<7>(ws, nxtW, Lb, 76, 90, w, l16);
      DRAIN();
      if (w == 0) poll(ws, poll_off(flm + 32 + p, flm + 32 + p, lane), true, tag);
      RAWBAR();
      TS(8 + Lr * 12 + 10);
    } else {
      GAS unsigned char* orow = (GAS unsigned char*)out + (16 * To + tok) * 1152 + 72 * go;
      *(GAS v2u*)(orow + 8 * gq) = xa;
      *(GAS v2u*)(orow + 40 + 8 * gq) = xb;
      if (gq == 0) *(GAS v2u*)(orow + 32) = xc;
      if (b == 0 && tid == 0) __hip_atomic_store((GAS u64*)(ws + WS_EPOCH), tag, RLX_AGENT);
      DRAIN();
      TS(8 + Lr * 12 + 9);
    }
  };
#pragma unroll 1
  for (int it = 0; it < 2; ++it) { layer(IC(0), it); layer(IC(1), it); }
}

extern "C" void kernel_launch(void* const* d_in, const int* in_sizes, int n_in, void* d_out, int out_size, void* d_ws, size_t ws_size, hipStream_t stream) {
  (void)hipFuncSetAttribute((const void*)fused_kernel, hipFuncAttributeMaxDynamicSharedMemorySize, L_TOTAL);
  fused_kernel<<<256, 512, L_TOTAL, stream>>>((const u16*)d_in[0], (const u16*)d_in[1], (const u16*)d_in[2], (const u16*)d_in[3],
                                              (const u16*)d_in[5], (const u16*)d_in[6], (u16*)d_out, (unsigned char*)d_ws);
}
```

```cpp
#include <hip/hip_runtime.h>
#include <type_traits>
#include <utility>
typedef unsigned short u16;
typedef unsigned long long u64;
typedef unsigned v4u __attribute__((ext_vector_type(4)));
typedef unsigned v2u __attribute__((ext_vector_type(2)));
typedef float f32x4 __attribute__((ext_vector_type(4)));
typedef float f32x2 __attribute__((ext_vector_type(2)));
typedef short bf16x8 __attribute__((ext_vector_type(8)));
typedef short s16x4 __attribute__((ext_vector_type(4)));
typedef __bf16 bf16x2 __attribute__((ext_vector_type(2)));
#define GAS __attribute__((address_space(1)))
#define LAS __attribute__((address_space(3)))
#define RLX_AGENT __ATOMIC_RELAXED, __HIP_MEMORY_SCOPE_AGENT
#define DEV __device__ __forceinline__
#define IC(n) std::integral_constant<int, (n)>{}

constexpr int WS_EPOCH = 0;
constexpr int WS_FLAGS = 4096;
constexpr int FL_RT1 = 0, FL_RT2 = 16, FL_HOP = 32;
constexpr int WQ_BLOB = 129024, WO_BLOB = 82944;
constexpr int WS_WQ = 1 << 20;
constexpr int WS_WO = WS_WQ + 2 * 16 * WQ_BLOB;
constexpr int FT = 20480;
constexpr int FRAG_BUF = 128 * FT;
constexpr int WS_FX = 8 << 20;
constexpr int WS_FO = WS_FX + 3 * FRAG_BUF;
constexpr int KVX_REC = 2304;
constexpr int KVX_BUF = 128 * 16 * KVX_REC;
constexpr int WS_KVX = WS_FO + 4 * FRAG_BUF;
constexpr int KVP_REC = 5760;
constexpr int WS_KVP = WS_KVX + 2 * KVX_BUF;
constexpr int WS_CSP = WS_KVP + 2 * 256 * KVP_REC;
constexpr int WS_DBG = WS_CSP + 2 * 16 * 18 * 448;
constexpr int WS_TOTAL = WS_DBG + 256 * 64 * 8 * 10;
static_assert(WS_WO + 2 * 8 * WO_BLOB <= WS_FX, "ws map");
constexpr int L_O = 77824, L_TOTAL = 163840;
constexpr int O_LATE = 50;
constexpr int L_STG = WQ_BLOB;
constexpr int L_XCH = 31 * 1024;
constexpr int IMG_REC = 2320, STG_REC = 3472;
constexpr int L_CS = L_O + WO_BLOB;
constexpr int L_PH = L_CS + 896;
constexpr int L_ONEQ = L_TOTAL - 16;
static_assert(L_PH + 1480 <= L_ONEQ && L_PH % 8 == 0 && L_O + O_LATE * 1024 == WQ_BLOB && 24 * IMG_REC <= 55 * 1024, "lds map");
constexpr int OOB = 0x7ffffff0;
constexpr unsigned SPIN_CAP = 1u << 14;
constexpr float LOG2E = 1.4426950408889634f;
constexpr float BIG = 18446744073709551616.0f;
constexpr int RING = 14;

DEV f32x4 mfma(v4u a, v4u b, f32x4 c) {
  return __builtin_amdgcn_mfma_f32_16x16x32_bf16(__builtin_bit_cast(bf16x8, a), __builtin_bit_cast(bf16x8, b), c, 0, 0, 0);
}
DEV unsigned pk2(float lo, float hi) { f32x2 v = {lo, hi}; return __builtin_bit_cast(unsigned, __builtin_convertvector(v, bf16x2)); }
DEV v2u pk4(f32x4 v) { return v2u{pk2(v.x, v.y), pk2(v.z, v.w)}; }
DEV unsigned fu(float f) { return __builtin_bit_cast(unsigned, f); }
DEV float uf(unsigned u) { return __builtin_bit_cast(float, u); }
DEV f32x4 unpk4(v2u u) { return f32x4{uf(u.x << 16), uf(u.x & 0xffff0000u), uf(u.y << 16), uf(u.y & 0xffff0000u)}; }
DEV float dot2(unsigned a, float c) { return __builtin_amdgcn_fdot2_f32_bf16(__builtin_bit_cast(bf16x2, a), __builtin_bit_cast(bf16x2, a), c, false); }
DEV v4u cat(v2u a, v2u b) { return v4u{a.x, a.y, b.x, b.y}; }
DEV float red_add(float x) { auto a = __builtin_amdgcn_permlane16_swap(fu(x), fu(x), false, false); x = uf(a[0]) + uf(a[1]); auto b = __builtin_amdgcn_permlane32_swap(fu(x), fu(x), false, false); return uf(b[0]) + uf(b[1]); }
DEV float red_max(float x) { auto a = __builtin_amdgcn_permlane16_swap(fu(x), fu(x), false, false); x = fmaxf(uf(a[0]), uf(a[1])); auto b = __builtin_amdgcn_permlane32_swap(fu(x), fu(x), false, false); return fmaxf(uf(b[0]), uf(b[1])); }
DEV float red_min(float x) { auto a = __builtin_amdgcn_permlane16_swap(fu(x), fu(x), false, false); x = fminf(uf(a[0]), uf(a[1])); auto b = __builtin_amdgcn_permlane32_swap(fu(x), fu(x), false, false); return fminf(uf(b[0]), uf(b[1])); }
DEV float odd_rows(float x) { auto a = __builtin_amdgcn_permlane16_swap(fu(x), fu(x), false, false); return uf(a[1]); }
DEV float hi_half(float x) { auto a = __builtin_amdgcn_permlane32_swap(fu(x), fu(x), false, false); return uf(a[1]); }

typedef LAS unsigned char* lptr;
DEV v4u lds128(lptr L, int off) { return *(LAS v4u*)(L + off); }
DEV v2u lds64(lptr L, int off) { return *(LAS v2u*)(L + off); }
DEV v2u ldstr(lptr L, int off) { return __builtin_bit_cast(v2u, __builtin_amdgcn_ds_read_tr16_b64_v4i16((LAS s16x4*)(L + off))); }
DEV void sts128(lptr L, int off, v4u v) { *(LAS v4u*)(L + off) = v; }
DEV void sts64(lptr L, int off, v2u v) { *(LAS v2u*)(L + off) = v; }
#define BAR() do { asm volatile("s_waitcnt lgkmcnt(0)" ::: "memory"); __builtin_amdgcn_s_barrier(); asm volatile("" ::: "memory"); } while (0)
#define RAWBAR() do { asm volatile("" ::: "memory"); __builtin_amdgcn_s_barrier(); asm volatile("" ::: "memory"); } while (0)
#define VMWAIT(n) asm volatile("s_waitcnt vmcnt(%0)" :: "n"(n) : "memory")
#define DRAIN() VMWAIT(0)
#define PIN() __builtin_amdgcn_sched_barrier(0)
#define PINM() __builtin_amdgcn_sched_barrier(0x406)
typedef __amdgpu_buffer_rsrc_t rsrc_t;
DEV v4u gld128(rsrc_t r, int off, int soff = 0) { return __builtin_amdgcn_raw_buffer_load_b128(r, off, soff, 16); }
DEV void gst128(rsrc_t r, int off, v4u v) { __builtin_amdgcn_raw_buffer_store_b128(v, r, off, 0, 16); }
DEV void gst64(rsrc_t r, int off, v2u v) { __builtin_amdgcn_raw_buffer_store_b64(v, r, off, 0, 16); }
template <int OFF> DEV void aload(v4u& d, const unsigned char* sbase, int voff) { asm volatile("global_load_dwordx4 %0, %1, %2 offset:%3 sc1" : "=v"(d) : "v"(voff), "s"(sbase), "n"(OFF) : "memory"); }
DEV void aload64(v2u& d, const unsigned char* sbase, int voff) { asm volatile("global_load_dwordx2 %0, %1, %2 sc1" : "=v"(d) : "v"(voff), "s"(sbase) : "memory"); }
DEV void aload64p(v2u& d, const void* sbase, int voff) { asm volatile("global_load_dwordx2 %0, %1, %2" : "=v"(d) : "v"(voff), "s"(sbase) : "memory"); }
#define GOT(n, reg) asm volatile("s_waitcnt vmcnt(%1)" : "+v"(reg) : "n"(n) : "memory")
DEV void dmaf(const unsigned char* ws, int blob, unsigned ldsb, int f, int l16) {
  unsigned keep, st; int vt;
  asm volatile("s_mov_b32 %0, m0\n\ts_lshl_b32 %1, %5, 10\n\ts_add_u32 m0, %1, %4\n\ts_add_u32 %1, %1, %7\n\tv_add_u32 %2, %1, %6\n\tglobal_load_lds_dwordx4 %2, %3 sc1\n\ts_mov_b32 m0, %0"
               : "=&s"(keep), "=&s"(st), "=&v"(vt) : "s"(ws), "s"(ldsb), "s"(f), "v"(l16), "s"(__builtin_amdgcn_readfirstlane(blob)) : "memory", "scc");
}
DEV void dma_in(const void* base, int voff, unsigned ldsdst) {
  unsigned keep;
  asm volatile("s_mov_b32 %0, m0\n\ts_mov_b32 m0, %3\n\ts_nop 0\n\tglobal_load_lds_dwordx4 %1, %2\n\ts_mov_b32 m0, %0" : "=&s"(keep) : "v"(voff), "s"(base), "s"(ldsdst) : "memory");
}
template <int NW> DEV void dma_range(const unsigned char* ws, int blob, unsigned ldsbase, int f0, int f1, int w, int l16) {
  if (NW == 7 && w == 0) return;
#pragma unroll 1
  for (int f = f0 + (NW == 7 ? w - 1 : w); f < f1; f += NW) dmaf(ws, blob, ldsbase, f, l16);
}
DEV int poll_off(int lineA, int lineB, int lane) { return WS_FLAGS + ((lane & 16) ? lineB : lineA) * 128 + (lane & 15) * 8; }
DEV void poll(const unsigned char* ws, int off, bool care, u64 tag) {
  for (unsigned spins = 0; spins < SPIN_CAP; ++spins) if (__all(!care || __hip_atomic_load((const GAS u64*)(ws + off), RLX_AGENT) == tag)) break;
  __builtin_amdgcn_fence(__ATOMIC_ACQUIRE, "wavefront");
}
DEV void set_flag(unsigned char* ws, int line, int idx, u64 tag) { __hip_atomic_store((GAS u64*)(ws + WS_FLAGS + line * 128) + idx, tag, RLX_AGENT); }
DEV int unit_row(int s, int i) {
  return s < 16 ? 36 * s + (i < 16 ? i : i + 4) : 36 * (8 * (s - 16) + 2 * ((i >> 2) & 3) + (i >> 4)) + 16 + (i & 3);
}
#define TS(i) do {} while (0)

template <bool SAFE> DEV bool phi_maps(const f32x4 (&pv)[2][2], int gq, v2u (&pl)[2], v2u (&ml)[2], v2u (&hq)[2]) {
  f32x4 ep[2], em[2]; float hp[2][2], hm[2][2], sp[2], sm[2];
#pragma unroll
  for (int h = 0; h < 2; ++h) {
    const f32x4 lo = pv[h][0], hi = pv[h][1];
    float mx, mn;
    if (SAFE) {
      mx = red_max(fmaxf(fmaxf(fmaxf(lo.x, lo.y), fmaxf(lo.z, lo.w)), fmaxf(hi.x, hi.y))) * LOG2E;
      mn = red_min(fminf(fminf(fminf(lo.x, lo.y), fminf(lo.z, lo.w)), fminf(hi.x, hi.y))) * LOG2E;
    } else mx = mn = hi.x * LOG2E;
#pragma unroll
    for (int r = 0; r < 4; ++r) { ep[h][r] = __builtin_amdgcn_exp2f(__builtin_fmaf(lo[r], LOG2E, -mx)); em[h][r] = __builtin_amdgcn_exp2f(-__builtin_fmaf(lo[r], LOG2E, -mn)); }
    hp[h][0] = SAFE ? __builtin_amdgcn_exp2f(__builtin_fmaf(hi.x, LOG2E, -mx)) : 1.0f;
    hm[h][0] = SAFE ? __builtin_amdgcn_exp2f(-__builtin_fmaf(hi.x, LOG2E, -mn)) : 1.0f;
    hp[h][1] = __builtin_amdgcn_exp2f(__builtin_fmaf(hi.y, LOG2E, -mx)); hm[h][1] = __builtin_amdgcn_exp2f(-__builtin_fmaf(hi.y, LOG2E, -mn));
    sp[h] = (ep[h].x + ep[h].y) + (ep[h].z + ep[h].w); sm[h] = (em[h].x + em[h].y) + (em[h].z + em[h].w);
  }
  auto a0 = __builtin_amdgcn_permlane16_swap(fu(sp[0]), fu(sm[0]), false, false);
  auto a1 = __builtin_amdgcn_permlane16_swap(fu(sp[1]), fu(sm[1]), false, false);
  auto a2 = __builtin_amdgcn_permlane32_swap(fu(uf(a0[0]) + uf(a0[1])), fu(uf(a1[0]) + uf(a1[1])), false, false);
  const float h0 = (gq & 1) ? hm[0][0] + hm[0][1] : hp[0][0] + hp[0][1], h1 = (gq & 1) ? hm[1][0] + hm[1][1] : hp[1][0] + hp[1][1];
  const float tot = (uf(a2[0]) + uf(a2[1])) + (gq < 2 ? h0 : h1);
  const float rc = __builtin_amdgcn_rcpf(tot);
  auto b0 = __builtin_amdgcn_permlane32_swap(fu(rc), fu(rc), false, false);
#pragma unroll
  for (int h = 0; h < 2; ++h) {
    auto b1 = __builtin_amdgcn_permlane16_swap(b0[h], b0[h], false, false);
    const float rp = uf(b1[0]), rm = uf(b1[1]);
    pl[h] = pk4(ep[h] * rp); ml[h] = pk4(em[h] * rm);
    hq[h] = v2u{pk2(hp[h][0] * rp, hp[h][1] * rp), pk2(hm[h][0] * rm, hm[h][1] * rm)};
  }
  return tot < BIG;
}
DEV void phi_pair(const f32x4 (&pv)[2][2], int gq, v2u (&pl)[2], v2u (&ml)[2], v2u (&hq)[2]) {
  if (__builtin_expect(!__all(phi_maps<false>(pv, gq, pl, ml, hq)), 0)) phi_maps<true>(pv, gq, pl, ml, hq);
}
constexpr int fr_s(int ty, int i) { return i >= 90 ? (i - 90) / 2 : ty == 1 ? i / 5 : i < 54 ? i / 3 : (i - 54) / 2; }
constexpr int fr_t(int ty, int i) { return i >= 90 ? 5 + (i - 90) % 2 : ty == 1 ? i % 5 : i < 54 ? (i % 3 == 2 ? 4 : i % 3) : 2 + (i - 54) % 2; }
__host__ __device__ constexpr int fr_slot(int i) { return i < 21 ? 55 + i : i < 76 ? i - 21 : i; }
constexpr int xn(int s) { return s < 0 ? 6 : s < 13 ? 7 + s + s / 2 : 25; }
constexpr int on(int u) { return u < 0 ? 6 : u < 7 ? 7 + u : 14; }
template <int lo, int hi, class F> DEV void steps(F&& f) {
  [&]<int... k>(std::integer_sequence<int, k...>) { (f(IC(lo + k)), ...); }(std::make_integer_sequence<int, hi - lo>{});
}

__global__ void __launch_bounds__(512, 2) fused_kernel(const u16* __restrict__ x0, const u16* __restrict__ wA, const u16* __restrict__ wphi, const u16* __restrict__ woA,
                                                       const u16* __restrict__ wB, const u16* __restrict__ woB, u16* __restrict__ out, unsigned char* ws0) {
  unsigned char* const ws = ws0;
  extern __shared__ __attribute__((aligned(16))) unsigned char smem[];
  const lptr L = (lptr)smem;
  const unsigned Lb = (unsigned)(uintptr_t)L;
  const int b = blockIdx.x, tid = threadIdx.x, lane0 = tid & 63, w0 = __builtin_amdgcn_readfirstlane(tid >> 6);
  const int p0 = ((b & 7) >> 1) * 4 + (b >> 6), g0 = (b & 1) * 8 + ((b >> 3) & 7);
  const rsrc_t rs = __builtin_amdgcn_make_buffer_rsrc((void*)ws, 0, WS_TOTAL, 0x00020000);
  TS(0);
  u64 tag;
  v4u xf[18];
  f32x4 res[3];
  v4u ring[15];
  float ssq;
  const f32x4 z = {0.f, 0.f, 0.f, 0.f};
  auto colsums7 = [&](int type) {
    int lane = lane0;
    asm volatile("" : "+v"(lane));
    const int base = WS_CSP + (type * 16 + g0) * 18 * 448 + (lane >> 5) * 9 * 448 + (lane & 31) * 16;
    v4u c[9];
#pragma unroll
    for (int i = 0; i < 9; ++i) c[i] = gld128(rs, (lane & 31) < 28 ? base + i * 448 : OOB);
    f32x4 t = __builtin_bit_cast(f32x4, c[0]);
#pragma unroll
    for (int i = 1; i < 9; ++i) t += __builtin_bit_cast(f32x4, c[i]);
#pragma unroll
    for (int r = 0; r < 4; ++r) { auto sw = __builtin_amdgcn_permlane32_swap(fu(t[r]), fu(t[r]), false, false); t[r] = uf(sw[0]) + uf(sw[1]); }
    if (lane < 28) *(LAS f32x4*)(L + L_CS + type * 448 + lane * 16) = t;
  };
  {
    const int lane = lane0, tok = lane & 15, gq = lane >> 4, w = w0, p = p0, g = g0, T = 8 * p + w;
    const int trq = (lane >> 2) & 3, trp = lane & 3;
    constexpr int RAW_A = 147456;
    constexpr int RAW_B = WQ_BLOB, RAW_OA = RAW_B + 6912, RAW_OB = RAW_OA + 2304, RAW_E1 = RAW_OB + 2304;
    const v4u ones = {0x3F803F80u, 0x3F803F80u, 0x3F803F80u, 0x3F803F80u};
    const int pc0 = tid, pc1 = tid + 512 > 863 ? 863 : tid + 512;
    const int i0 = pc0 / 27, q0 = pc0 - i0 * 27, i1 = pc1 / 27, q1 = pc1 - i1 * 27;
    const int co0 = (q0 / 9) * 1080 + q0 * 8 + g * 72, co1 = (q1 / 9) * 1080 + q1 * 8 + g * 72;
    auto wq_load = [&](const u16* W, int s, v2u (&d)[2]) { aload64p(d[0], W, unit_row(s, i0) * 3456 + co0); aload64p(d[1], W, unit_row(s, i1) * 3456 + co1); };
    auto wq_put = [&](int img, const v2u (&d)[2]) { sts64(L, img + pc0 * 8, d[0]); sts64(L, img + pc1 * 8, d[1]); };
    const int wco = (w & 1) * 40 + (w < 2 ? 72 : w < 4 ? 144 : 0) - (w > 4 ? 40 : 0) + (w == 6 ? 80 : 0);
    auto wq_emit = [&](int img, int type, int s) {
      if (w < 7) {
        int a0 = img + (4 * gq + trq) * 216 + (w == 4 ? trp * 72 + 32 : wco + trp * 8), a1 = a0 + 16 * 216;
        if (w == 4 && trp == 3) a0 = a1 = L_ONEQ;
        const int j = w >= 5 ? 90 + 2 * s + w - 5 : type == 1 ? 5 * s + w : (w == 2 || w == 3) ? 54 + 2 * s + w - 2 : 3 * s + (w >> 1 ? 2 : w);
        const v4u f = cat(ldstr(L, a0), ldstr(L, a1));
        gst128(rs, WS_WQ + (type * 16 + g) * WQ_BLOB + fr_slot(j) * 1024 + lane * 16, f);
        gst128(rs, tok == 0 ? WS_CSP + ((type * 16 + g) * 18 + s) * 448 + w * 64 + gq * 16 : OOB, __builtin_bit_cast(v4u, mfma(f, ones, z)));
      }
    };
    const int po = tid > 287 ? 287 : tid, io = po / 9;
    auto wo_load = [&](const u16* W, int s, v2u& d) { aload64p(d, W, unit_row(s, io) * 1152 + g * 72 + (po - io * 9) * 8); };
    auto wo_emit = [&](int img, int type, int s, int t) {
      const int a0 = img + (4 * gq + trq) * 72 + (t == 2 ? 32 : t * 40 + trp * 8);
      const int idx = s < 16 ? 2 * (s & 7) + 2 + (s >> 3) : s - 16;
      const int o = WS_WO + (type * 8 + (g >> 1)) * WO_BLOB + (17 - idx) * 4608 + (t < 2 ? (g & 1) * 2048 + t * 1024 + lane * 16 : 4096 + (gq * 8 + (g & 1) * 4 + tok) * 16);
      gst128(rs, (t == 2 && tok >= 4) ? OOB : o, cat(ldstr(L, a0), ldstr(L, a0 + 16 * 72)));
    };
    v2u dA[2], dE[2], dB[2], dO[3], r0, r1, r2, ep, dP;
    const int xs = 16 + (p & 1);
    wq_load(wA, p, dA);
    wq_load(p < 2 ? wA : wB, xs, dE);
    aload64(ep, ws, WS_EPOCH);
    PIN();
    auto x_dma = [&](auto i_) {
      constexpr int i = decltype(i_)::value;
      const int ci = i * 64 + lane, r = (ci * 911) >> 16;
      const int src = T * 18432 + ci * 16 - r * 16 + (ci - r * 72 < r ? 1152 : 0);
      dma_in(x0, src, Lb + w * 18432 + i * 1024);
    };
    steps<0, 6>(x_dma);
    if (tid == 0) sts64(L, L_ONEQ, v2u{0x3F80u, 0u});
    asm volatile("s_waitcnt vmcnt(6)" : "+v"(dA[0]), "+v"(dA[1]), "+v"(dE[0]), "+v"(dE[1]), "+v"(ep) :: "memory");
    wq_put(RAW_A, dA);
    tag = ((u64)(unsigned)__builtin_amdgcn_readfirstlane(ep.x) | ((u64)(unsigned)__builtin_amdgcn_readfirstlane(ep.y) << 32)) + 1;
    BAR();
    wq_emit(RAW_A, 0, p);
    if (p < 2) { BAR(); wq_put(RAW_A, dE); BAR(); wq_emit(RAW_A, 0, xs); }
    PIN();
    steps<6, 12>(x_dma);
    VMWAIT(6);
    RAWBAR();
    if (tid == 448) set_flag(ws, FL_RT1 + g, p, tag);
    TS(1);
    steps<12, 18>(x_dma);
    wq_load(wB, p, dB); wo_load(woA, p, dO[0]); wo_load(woB, p, dO[1]); wo_load(p < 6 ? woA : woB, xs, dO[2]);
    const int xr = (16 * (8 * p + 4 * (g & 1) + (w & 3)) + tok) * 1152 + 72 * ((g & ~1) + (w >> 2));
    aload64p(r0, x0, xr + 8 * gq); aload64p(r1, x0, xr + 40 + 8 * gq); aload64p(r2, x0, xr + 32);
    const int phi_i = tid < 162 ? tid : 161;
    aload64p(dP, wphi, g * 1296 + phi_i * 8);
    VMWAIT(9);
    {
      const int xb = w * 18432 + tok * 1152;
      auto xoff = [&](int byte) { int c = (byte >> 4) + tok; c -= c >= 72 ? 72 : 0; return xb + c * 16 + (byte & 8); };
#pragma unroll
      for (int s = 0; s < 16; ++s) xf[s] = cat(lds64(L, xoff(72 * s + 8 * gq)), lds64(L, xoff(72 * s + 40 + 8 * gq)));
#pragma unroll
      for (int s = 0; s < 2; ++s) xf[16 + s] = cat(lds64(L, xoff(72 * (8 * s + 2 * gq) + 32)), lds64(L, xoff(72 * (8 * s + 2 * gq + 1) + 32)));
    }
    TS(2);
    if (w == 0) poll(ws, poll_off(FL_RT1 + g, FL_RT1 + g, lane), true, tag);
    asm volatile("s_waitcnt vmcnt(0)" : "+v"(dB[0]), "+v"(dB[1]), "+v"(dO[0]), "+v"(dO[1]), "+v"(dO[2]), "+v"(r0), "+v"(r1), "+v"(r2), "+v"(dP) :: "memory");
    BAR();
    TS(3);
    if (w == 0) colsums7(0);
    dma_range<7>(ws, WS_WQ + g * WQ_BLOB, Lb, 0, 126, w, lane * 16);
    wq_put(RAW_B, dB); sts64(L, RAW_OA + po * 8, dO[0]); sts64(L, RAW_OB + po * 8, dO[1]);
    if (p >= 2 && p < 4) wq_put(RAW_E1, dE);
    else if (p >= 4 && p < 8) sts64(L, RAW_E1 + po * 8, dO[2]);
    { const int d0 = 2 * phi_i, d1 = d0 + 1; *(LAS unsigned*)(L + L_PH + (d0 / 9) * 4 + d0 * 4) = dP.x; *(LAS unsigned*)(L + L_PH + (d1 / 9) * 4 + d1 * 4) = dP.y; }
    if (tid >= 448 && tid < 448 + 46) { const int i = tid - 448; *(LAS unsigned*)(L + L_PH + (i < 36 ? i * 40 + 36 : 1440 + (i - 36) * 4)) = 0u; }
    res[0] = unpk4(r0); res[1] = unpk4(r1); res[2] = unpk4(r2);
    {
      float q4[4] = {0.f, 0.f, 0.f, 0.f};
#pragma unroll
      for (int s = 0; s < 18; ++s)
#pragma unroll
        for (int j = 0; j < 4; ++j) q4[j] = dot2(xf[s][j], q4[j]);
      ssq = (q4[0] + q4[1]) + (q4[2] + q4[3]);
    }
    DRAIN();
    BAR();
    TS(4);
    wq_emit(RAW_B, 1, p);
    if (p >= 2 && p < 4) wq_emit(RAW_E1, 1, xs);
    if (w < 3) wo_emit(RAW_OA, 0, p, w); else if (w < 6) wo_emit(RAW_OB, 1, p, w - 3);
    if (p >= 4 && p < 8 && w < 3) wo_emit(RAW_E1, p < 6 ? 0 : 1, xs, w);
#pragma unroll
    for (int i = 0; i < RING; ++i) ring[i] = lds128(L, fr_slot(i) * 1024 + lane * 16);
    PIN();
  }

  auto layer = [&](auto ty_, const int it) __attribute__((always_inline)) {
    constexpr int ty = decltype(ty_)::value;
    const int Lr = 2 * it + ty;
    int lane = lane0, w = w0, p = p0, g = g0;
    unsigned char* ws = ws0;
    asm volatile("" : "+v"(lane), "+s"(w), "+s"(p), "+s"(g), "+s"(ws));
    const int T = 8 * p + w;
    const int tok = lane & 15, gq = lane >> 4, trq = (lane >> 2) & 3, trp = lane & 3, l16 = lane * 16;
    const bool last = (ty == 1) && (it == 1);
    int hi16 = l16 + 65536;
    asm volatile("" : "+v"(hi16));
    auto wfrag = [&](int slot) { return slot < 64 ? lds128(L, l16 + slot * 1024) : lds128(L, hi16 + (slot - 64) * 1024); };
    const int curW = WS_WQ + (ty * 16 + g) * WQ_BLOB, nxtW = WS_WQ + ((1 - ty) * 16 + g) * WQ_BLOB, curO = WS_WO + (ty * 8 + (g >> 1)) * WO_BLOB;
    const int flm = FL_HOP + Lr * 48;
    v4u rv[4];
    const int xtb = WS_FX + (Lr - 1) * FRAG_BUF + T * FT, xv0 = xtb + 4096 + l16, xv1 = xv0 + 8192, xvr = xtb + 16384 + gq * 512 + tok * 16;
    auto x_op = [&](auto k_) {
      constexpr int k = decltype(k_)::value;
      if constexpr (k < 16) aload<(k % 8) * 1024 - 4096>(xf[k], ws, k < 8 ? xv0 : xv1);
      else if constexpr (k < 20) aload<((k - 16) & 1) * 256 + ((k - 16) >> 1) * 2048>(rv[k - 16], ws, xvr);
      else dmaf(ws, curW, Lb, min(90 + (k - 20) * 8 + w, 125), l16);
    };
    if (Lr > 0) steps<0, xn(-1)>(x_op);
    PIN();
    f32x4 acc[7];
#pragma unroll
    for (int t = 0; t < 7; ++t) acc[t] = z;
    constexpr int QB = ty == 0 ? 72 : 88;
    auto qkv_step = [&](auto i_) {
      constexpr int i = decltype(i_)::value, s = fr_s(ty, i), t = fr_t(ty, i), P1 = ty == 0 ? 3 : 5;
      if (i < 16 * P1 && t == 0) { PIN(); if (Lr > 0) steps<xn(s - 1), xn(s)>(x_op); PIN(); GOT(xn(s) - s - 1, xf[s]); }
      if (i == 16 * P1) {
        PIN();
        asm volatile("s_waitcnt vmcnt(5)" : "+v"(rv[0]), "+v"(rv[1]), "+v"(rv[2]), "+v"(rv[3]) :: "memory");
        if (Lr > 0) { xf[16] = v4u{rv[0].x, rv[0].y, rv[1].x, rv[1].y}; xf[17] = v4u{rv[2].x, rv[2].y, rv[3].x, rv[3].y}; ssq = (uf(rv[0].z) + uf(rv[1].z)) + (uf(rv[2].z) + uf(rv[3].z)); }
      }
      if (i == QB) {
        DRAIN(); RAWBAR();
#pragma unroll
        for (int j = 90; j < QB + RING; ++j) ring[j % RING] = wfrag(fr_slot(j));
      }
      acc[t] = mfma(ring[i % RING], xf[s], acc[t]);
      if (i + RING < 126 && (i + RING < 90 || i >= QB)) ring[i % RING] = wfrag(fr_slot(i + RING));
      if (ty == 0 && i >= 54 && i < QB) {
        __builtin_amdgcn_sched_group_barrier(0x008, 1, 0); __builtin_amdgcn_sched_group_barrier(0x100, 1, 0);
        __builtin_amdgcn_sched_group_barrier(0x002, 6, 0);
      } else PINM();
    };
    const int csb = L_CS + ty * 448 + gq * 16;
    auto wph = [&](int h, int ft) {
      const int rl = L_PH + (20 * h + 4 * gq + trq) * 40, ru = L_PH + ((trq >> 1) == h ? (18 * h + (h ? 0 : 16) + (trq & 1)) * 40 : 1440);
      return cat(ldstr(L, rl + (ft ? 32 : trp * 8)), ldstr(L, ru + (ft ? 32 : trp * 8)));
    };
    const int vsw = gq * 128 + ((tok * 8) ^ ((gq >> 1) * 64));
    const int trl = trp * 128 + (((4 * gq + trq) * 8) ^ ((trp >> 1) * 64));
    steps<0, ty == 0 ? 54 : 90>(qkv_step);
    if (Lr == 0) {
      DRAIN();
      RAWBAR();
      if (tid == 448) set_flag(ws, FL_RT2 + g, p, tag);
    }
    const float mean = hi_half(odd_rows(acc[4].x)) * (1.0f / 576.0f);
    const float var = red_add(ssq) * (1.0f / 576.0f) - mean * mean;
    const float rstd = __builtin_amdgcn_rsqf(fmaxf(var, 0.f) + 1e-5f);
    auto fix = [&](int t, float sc) { return (acc[t] - mean * *(LAS f32x4*)(L + csb + t * 64)) * sc; };
    v2u kq[2], vq[2];
    kq[0] = pk4(fix(0, rstd)); kq[1] = pk4(fix(1, rstd));
    const f32x4 remf = fix(4, rstd);
    const v2u remq = pk4(remf);
    const int vpos = gq * 256 + ((tok * 16) ^ ((gq >> 1) * 128));
    v4u kboth, vboth;
    const int kh = w >= 3 ? 1 : 0, kft = w - 3 * kh;
    if (ty == 0) {
      const v2u ku = gq == 1 ? remq : v2u{0u, 0u};
      f32x4 pv[2][2];
#pragma unroll
      for (int h = 0; h < 2; ++h) { const v4u bk = cat(kq[h], ku); pv[h][0] = mfma(wph(h, 0), bk, z); pv[h][1] = mfma(wph(h, 1), bk, z); }
      PIN();
      v2u pl[2], ml[2], hq[2];
      const bool ok = phi_maps<false>(pv, gq, pl, ml, hq);
      steps<54, QB>(qkv_step);
      PIN();
      if (__builtin_expect(!__all(ok), 0)) phi_maps<true>(pv, gq, pl, ml, hq);
      const int sb = L_STG + w * STG_REC;
      if (gq == 2) sts64(L, sb + 3072 + tok * 8, remq);
      if (lane >= 62) sts64(L, sb + 3456 + (lane & 1) * 8, v2u{(lane & 1) ? 0u : 0x3F80u, 0u});
#pragma unroll
      for (int h = 0; h < 2; ++h) {
        sts64(L, sb + 1024 + h * 1024 + vsw, pl[h]); sts64(L, sb + 1536 + h * 1024 + vsw, ml[h]);
        if (gq == 0) sts64(L, sb + 3200 + h * 128 + tok * 8, hq[h]);
      }
      PIN();
      steps<QB, 90>(qkv_step);
      vq[0] = pk4(fix(2, rstd)); vq[1] = pk4(fix(3, rstd));
      TS(8 + Lr * 12 + 0);
      sts64(L, sb + vsw, vq[0]); sts64(L, sb + 512 + vsw, vq[1]);
      BAR();
      if (w < 6) {
        const int aa = L_STG + (kft < 2 ? 1024 + kh * 1024 + kft * 512 + trl : (trp == 0 ? 3200 + kh * 128 + (4 * gq + trq) * 8 : 3464));
        const int bl = L_STG + kh * 512 + trl, bh = L_STG + (trp == 0 ? 3072 + (4 * gq + trq) * 8 : 3456);
        v4u fa[4], fl[4], fh[4];
#pragma unroll
        for (int ks = 0; ks < 4; ++ks) {
          const int o0 = 2 * ks * STG_REC, o1 = o0 + STG_REC;
          fa[ks] = cat(ldstr(L, aa + o0), ldstr(L, aa + o1)); fl[ks] = cat(ldstr(L, bl + o0), ldstr(L, bl + o1)); fh[ks] = cat(ldstr(L, bh + o0), ldstr(L, bh + o1));
        }
        PIN();
        f32x4 dl = z, dh = z;
#pragma unroll
        for (int ks = 0; ks < 4; ++ks) { dl = mfma(fa[ks], fl[ks], dl); dh = mfma(fa[ks], fh[ks], dh); }
        const int rb = WS_KVP + ((it * 16 + g) * 16 + p) * KVP_REC + kh * 2880;
        const int slot = tok == 4 ? 2 : (tok >> 1) == kh ? (tok & 1) : -1;
        const bool rowok = kft < 2 || gq == 0;
        if (rowok) gst128(rs, rb + kft * 1024 + l16, __builtin_bit_cast(v4u, dl));
        if (rowok && slot >= 0) gst128(rs, rb + 2304 + kft * 256 + (gq * 4 + slot) * 16, __builtin_bit_cast(v4u, dh));
      } else {
#pragma unroll
        for (int j = 0; j < 4; ++j) { unsigned zz = 0u; asm volatile("" : "+v"(zz)); sts128(L, (w - 6) * 4096 + j * 1024 + l16, v4u{zz, zz, zz, zz}); }
        if (it == 0 && w == 6) { poll(ws, poll_off(FL_RT2 + g, FL_RT2 + (g ^ 1), lane), true, tag); colsums7(1); }
      }
    } else {
      const int rb = WS_KVX + it * KVX_BUF + (T * 16 + g) * KVX_REC;
      gst128(rs, rb + l16, kboth = (gq & 1) ? cat(kq[1], kq[0]) : cat(kq[0], kq[1]));
      if (gq == 1 || gq == 2) gst64(rs, rb + 2048 + (gq - 1) * 128 + tok * 8, remq);
      vq[0] = pk4(fix(2, rstd)); vq[1] = pk4(fix(3, rstd));
      TS(8 + Lr * 12 + 0);
      gst128(rs, rb + 1024 + vpos, vboth = (gq & 1) ? cat(vq[1], vq[0]) : cat(vq[0], vq[1]));
    }
    PIN();
    steps<90, 108>(qkv_step);
    DRAIN();
    RAWBAR();
    if (tid == 448) set_flag(ws, flm + g, p, tag);
    TS(8 + Lr * 12 + 1);
    if (ty == 1) {
      const int ib = (16 + w) * IMG_REC;
      sts128(L, ib + l16, kboth); sts128(L, ib + 1024 + vpos, vboth);
      if (gq == 1 || gq == 2) sts64(L, ib + 2048 + (gq - 1) * 128 + tok * 8, remq);
      if (lane < 3) { const int i = w + 8 * lane; sts128(L, i * IMG_REC + 2304, v4u{0x3F80u, 0u, (8 * (p - 2) + i >= 0) ? 0u : 0xF149u, 0u}); }
    } else dma_range<7>(ws, nxtW, Lb, 55, 76, w, l16);
    dma_range<7>(ws, curO, Lb + L_O, O_LATE, 81, w, l16);
    PIN();
    steps<108, 126>(qkv_step);
    constexpr float QS = 0.23570226039551584f * LOG2E;
    const v2u q0 = pk4(fix(5, ty == 1 ? rstd * QS : rstd)), q1 = pk4(fix(6, ty == 1 ? rstd * QS : rstd));
    v2u olo[2]; unsigned ohi[2];
    v2u pollv;
    auto poll_done = [&](bool care) {
      if (w == 0) {
        DRAIN(); PIN();
        asm volatile("" : "+v"(pollv));
        if (!__all(!care || ((u64)pollv.y << 32 | pollv.x) == tag)) poll(ws, poll_off(flm + g, flm + g, lane), care, tag);
      }
    };
    if (ty == 0) {
      v4u qf[2][2];
      {
        const v2u qu = gq == 0 ? remq : v2u{0u, 0u};
        f32x4 pv[2][2];
#pragma unroll
        for (int h = 0; h < 2; ++h) { const v4u bq = cat(h ? q1 : q0, qu); pv[h][0] = mfma(wph(h, 0), bq, z); pv[h][1] = mfma(wph(h, 1), bq, z); }
        PIN();
        if (w == 0) aload64(pollv, ws, poll_off(flm + g, flm + g, lane));
        PIN();
        v2u pl[2], ml[2], hq[2];
        phi_pair(pv, gq, pl, ml, hq);
#pragma unroll
        for (int h = 0; h < 2; ++h) { qf[h][0] = cat(pl[h], ml[h]); qf[h][1] = cat(hq[h], v2u{0u, 0u}); }
      }
      PIN();
      TS(8 + Lr * 12 + 2);
      poll_done(true);
      RAWBAR();
      TS(8 + Lr * 12 + 3);
      const int pi = w * 64 + lane;
      v4u dp[16];
      if (w < 6) {
        const int po = pi < 360 ? WS_KVP + (it * 16 + g) * 16 * KVP_REC + pi * 16 : OOB;
#pragma unroll
        for (int q = 0; q < 16; ++q) dp[q] = gld128(rs, po, q * KVP_REC);
        PIN();
        f32x4 s0 = __builtin_bit_cast(f32x4, dp[0]), s1 = __builtin_bit_cast(f32x4, dp[1]);
#pragma unroll
        for (int q = 2; q < 16; q += 2) { s0 += __builtin_bit_cast(f32x4, dp[q]); s1 += __builtin_bit_cast(f32x4, dp[q + 1]); }
        const v2u sv = pk4(s0 + s1);
        const int h = pi >= 180 ? 1 : 0, j = pi - 180 * h;
        const int e = j - 144, sl = e & 3, half = j < 144 ? (j >> 6) & 1 : (e >> 4) & 1;
        const int blk = j < 128 ? 0 : j < 144 ? 1024 : e < 32 ? 2048 : 3072;
        const int ln = j < 144 ? (j & 63) : ((e >> 2) & 3) * 16 + (sl < 2 ? 2 * h + sl : 4);
        const int dst = h * 4096 + blk + ln * 16 + half * 8;
        if (pi < 360 && (j < 144 || sl < 3)) sts64(L, dst, sv);
        if (pi < 360 && j >= 144 && sl == 2) sts64(L, dst + 128, sv);
      }
      BAR();
      TS(8 + Lr * 12 + 4);
      v4u kf[2][4];
#pragma unroll
      for (int h = 0; h < 2; ++h)
#pragma unroll
        for (int j = 0; j < 4; ++j) kf[h][j] = lds128(L, h * 4096 + j * 1024 + l16);
      PIN();
#pragma unroll
      for (int h = 0; h < 2; ++h) {
        const f32x4 dl2 = mfma(kf[h][1], qf[h][1], mfma(kf[h][0], qf[h][0], z));
        const f32x4 dh2 = mfma(kf[h][3], qf[h][1], mfma(kf[h][2], qf[h][0], z));
        const float r = __builtin_amdgcn_rcpf(odd_rows(dh2.x) + 1e-6f);
        olo[h] = pk4(dl2 * r); ohi[h] = h == 0 ? pk2(dh2.x * r, dh2.y * r) : pk2(dh2.z * r, dh2.w * r);
      }
    } else {
      v4u qf[2];
      {
        const v2u qr = pk4(remf * QS);
        const unsigned bias1 = gq == 1 ? 0x3F80u : 0u;
        qf[0] = cat(q0, v2u{gq == 0 ? qr.x : bias1, 0u});
        qf[1] = cat(q1, v2u{bias1, gq == 0 ? qr.y : 0u});
      }
      const int wb = w * IMG_REC;
      const int kuo = wb + (gq == 1 ? 2312 : 2048 + tok * 8);
      auto kfrag = [&](int h, int i) { return cat(lds64(L, wb + ((h ^ (gq & 1)) * 8) + l16 + i * IMG_REC), lds64(L, kuo + i * IMG_REC)); };
      PIN();
      if (w == 0) aload64(pollv, ws, poll_off(flm + g, flm + g, lane));
      PIN();
      float nm[2];
#pragma unroll
      for (int h = 0; h < 2; ++h) {
        const f32x4 sd = mfma(cat(kq[h], gq == 1 ? v2u{0u, 0u} : lds64(L, kuo + 16 * IMG_REC)), qf[h], z);
        float m = (4 * gq <= tok) ? sd.x : -3e38f;
#pragma unroll
        for (int r = 1; r < 4; ++r) m = fmaxf(m, (4 * gq + r <= tok) ? sd[r] : -3e38f);
        nm[h] = -red_max(m);
      }
      const int vl = wb + 1024 + trp * 256 + (((4 * gq + trq) * 16) ^ ((trp >> 1) * 128));
      const int vh = wb + (trp == 0 ? 2176 + (4 * gq + trq) * 8 : 2304);
      const unsigned m0 = (4 * gq + 0 <= tok ? 0xffffu : 0u) | (4 * gq + 1 <= tok ? 0xffff0000u : 0u);
      const unsigned m1 = (4 * gq + 2 <= tok ? 0xffffu : 0u) | (4 * gq + 3 <= tok ? 0xffff0000u : 0u);
      PIN();
      TS(8 + Lr * 12 + 2);
      poll_done(tok == p - 1 || tok == p - 2);
      RAWBAR();
      TS(8 + Lr * 12 + 3);
#pragma unroll
      for (int j = 0; j < 2; ++j) {
        const int Tg = 8 * (p - 2) + w + 8 * j, ib = (w + 8 * j) * IMG_REC;
        if (Tg >= 0) {
          const int rb = WS_KVX + it * KVX_BUF + (Tg * 16 + g) * KVX_REC;
          dmaf(ws, rb, Lb + ib, 0, l16); dmaf(ws, rb, Lb + ib, 1, l16);
          if (lane < 16) dmaf(ws, rb, Lb + ib, 2, l16);
        } else {
          unsigned zz = 0u; asm volatile("" : "+v"(zz));
          sts128(L, ib + l16, v4u{zz, zz, zz, zz}); sts128(L, ib + 1024 + l16, v4u{zz, zz, zz, zz});
          if (lane < 16) sts128(L, ib + 2048 + l16, v4u{zz, zz, zz, zz});
        }
      }
      DRAIN();
      BAR();
      TS(8 + Lr * 12 + 4);
#pragma unroll 1
      for (int pass = 0; pass < 2; ++pass) {
        v4u kf[34]; f32x4 sc[2][17]; v4u va, vb, van, vbn; f32x4 dl, dh;
        bool ok = true;
        constexpr int ord[17] = {16, 0, 1, 2, 3, 4, 5, 6, 7, 8, 9, 10, 11, 12, 13, 14, 15};
#pragma unroll
        for (int j = 0; j < 8; ++j) kf[j] = kfrag(0, ord[j]);
        auto vread = [&](int h, int ks) {
          const int i0 = ks == 0 ? 0 : 2 * ks - 1, i1 = ks == 0 ? 16 : ks < 8 ? 2 * ks : 15, vo = vl + ((h ^ (trp & 1)) * 8);
          van = cat(ldstr(L, vo + i0 * IMG_REC), ldstr(L, vo + i1 * IMG_REC));
          vbn = cat(ldstr(L, vh + i0 * IMG_REC), ldstr(L, vh + i1 * IMG_REC));
        };
        auto qk = [&](int j) { const int h = j / 17; sc[h][ord[j % 17]] = mfma(kf[j], qf[h], f32x4{nm[h], nm[h], nm[h], nm[h]}); };
        auto pexp = [&](f32x4 x) {
          f32x4 e;
#pragma unroll
          for (int r = 0; r < 4; ++r) e[r] = __builtin_amdgcn_exp2f(x[r]);
          return pk4(e);
        };
        vread(0, 0);
        PIN();
#pragma unroll
        for (int j = 0; j < 4; ++j) qk(j);
        PIN();
        auto att_step = [&](auto n_) {
          constexpr int n = decltype(n_)::value, h = n / 9, ks = n % 9;
          va = van; vb = vbn;
          if (n < 17) vread((n + 1) / 9, (n + 1) % 9);
          if (2 * n + 8 < 34) kf[2 * n + 8] = kfrag((2 * n + 8) / 17, ord[(2 * n + 8) % 17]);
          if (2 * n + 9 < 34) kf[2 * n + 9] = kfrag((2 * n + 9) / 17, ord[(2 * n + 9) % 17]);
          if (2 * n + 4 < 34) qk(2 * n + 4);
          if (2 * n + 5 < 34) qk(2 * n + 5);
          v4u bp;
          if (ks == 0) {
            f32x4 mg;
#pragma unroll
            for (int r = 0; r < 4; ++r) mg[r] = (4 * gq + r <= tok) ? sc[h][16][r] : sc[h][0][r];
            const v2u p0 = pexp(mg);
            bp = v4u{p0.x & ~m0, p0.y & ~m1, p0.x & m0, p0.y & m1};
          } else bp = cat(pexp(sc[h][2 * ks - 1]), ks < 8 ? pexp(sc[h][ks < 8 ? 2 * ks : 0]) : v2u{0u, 0u});
          dl = mfma(va, bp, ks == 0 ? z : dl); dh = mfma(vb, bp, ks == 0 ? z : dh);
          int wo_ = w; asm volatile("" : "+s"(wo_));
          if (n < 7) dmaf(ws, curO, Lb + L_O, min(n * 8 + wo_, O_LATE - 1), l16);
          else if (n < 10 && !last) dmaf(ws, nxtW, Lb, min(55 + (n - 7) * 8 + wo_, 75), l16);
          if (ks == 8) {
            const float l = odd_rows(dh.x);
            ok = ok && l < BIG;
            const float r = __builtin_amdgcn_rcpf(l);
            olo[h] = pk4(dl * r); ohi[h] = h == 0 ? pk2(dh.x * r, dh.y * r) : pk2(dh.z * r, dh.w * r);
          }
          PIN();
        };
        steps<0, 18>(att_step);
        if (__builtin_expect(__all(ok), 1)) break;
#pragma unroll
        for (int h = 0; h < 2; ++h) {
          const f32x4 s0 = mfma(kfrag(h, 0), qf[h], z), s16 = mfma(kfrag(h, 16), qf[h], z);
          float m = -3e38f;
#pragma unroll
          for (int r = 0; r < 4; ++r) m = fmaxf(m, (4 * gq + r <= tok) ? s16[r] : s0[r]);
#pragma unroll 1
          for (int i = 1; i < 16; ++i) { const f32x4 s = mfma(kfrag(h, i), qf[h], z); m = fmaxf(fmaxf(fmaxf(m, s.x), fmaxf(s.y, s.z)), s.w); }
          nm[h] = -red_max(m);
        }
      }
    }
    TS(8 + Lr * 12 + 5);
    gst128(rs, WS_FO + Lr * FRAG_BUF + T * FT + g * 1024 + l16, cat(olo[0], olo[1]));
    gst128(rs, gq == 0 ? WS_FO + Lr * FRAG_BUF + T * FT + 16384 + g * 256 + tok * 16 : OOB, v4u{ohi[0], ohi[1], 0u, 0u});
    const int kw = w >> 2, To = 8 * p + 4 * (g & 1) + (w & 3), go = (g & ~1) + kw;
    int omine = L_O + (1 - kw) * 4608 + kw * 2048 + l16, oother = L_O + (1 - kw) * 4608 + (1 - kw) * 2048 + l16, orem = L_O + (1 - kw) * 4608 + 4096 + (gq * 8 + (tok & 7)) * 16;
    asm volatile("" : "+v"(omine), "+v"(oother), "+v"(orem));
    auto ofrag = [&](int i) { const int u = i / 5, t = i % 5; return lds128(L, (8 - u) * 9216 + (t < 2 ? omine + t * 1024 : t < 4 ? oother + (t - 2) * 1024 : orem)); };
#pragma unroll
    for (int i = 0; i < 15; ++i) ring[i] = ofrag(i);
    PIN();
    DRAIN();
    RAWBAR();
    if (tid == 448) set_flag(ws, flm + 16 + p, g, tag);
    TS(8 + Lr * 12 + 6);
    if (ty == 0) { dma_range<7>(ws, curO, Lb + L_O, 0, O_LATE, w, l16); DRAIN(); }
    if (w == 0) poll(ws, poll_off(flm + 16 + p, flm + 16 + p, lane), true, tag);
    RAWBAR();
    TS(8 + Lr * 12 + 7);
    const int ob = WS_FO + Lr * FRAG_BUF + To * FT, ov0 = ob + kw * 8192 + 4096 + l16, ovr = ob + 16384 + kw * 2048 + gq * 512 + tok * 16;
    auto o_op = [&](auto k_) {
      constexpr int k = decltype(k_)::value;
      if constexpr (k < 2) aload<k * 256>(rv[k], ws, ovr);
      else if constexpr (k < 10) aload<(k - 2) * 1024 - 4096>(xf[k - 1], ws, ov0);
      else dmaf(ws, nxtW, Lb, min((k - 10) * 8 + w, 30), l16);
    };
    steps<0, on(-1)>(o_op);
    PIN();
    f32x4 oa[5] = {res[0], res[1], z, z, z};
    auto wo_step = [&](auto i_) {
      constexpr int i = decltype(i_)::value, u = i / 5, t = i % 5;
      if (t == 0) {
        PIN(); steps<on(u - 1), on(u)>(o_op); PIN();
        if (u == 0) {
          asm volatile("s_waitcnt vmcnt(%2)" : "+v"(rv[0]), "+v"(rv[1]) : "n"(on(0) - 2) : "memory");
          xf[0] = v4u{rv[0].x, rv[0].y, rv[1].x, rv[1].y};
        } else GOT(on(u) - u - 2, xf[u]);
      }
      oa[t] = mfma(ring[i % 15], xf[u], oa[t]);
      if (i + 15 < 45) ring[i % 15] = ofrag(i + 15);
      PINM();
    };
    steps<0, 45>(wo_step);
    {
      const int xm = L_XCH + w * 3072 + l16, xo = L_XCH + (w ^ 4) * 3072 + l16;
      sts128(L, xm, __builtin_bit_cast(v4u, oa[2])); sts128(L, xm + 1024, __builtin_bit_cast(v4u, oa[3])); sts128(L, xm + 2048, __builtin_bit_cast(v4u, oa[4]));
      BAR();
      res[0] = oa[0] + __builtin_bit_cast(f32x4, lds128(L, xo)); res[1] = oa[1] + __builtin_bit_cast(f32x4, lds128(L, xo + 1024));
      f32x4 rm = oa[4] + __builtin_bit_cast(f32x4, lds128(L, xo + 2048));
#pragma unroll
      for (int r = 0; r < 4; ++r) { auto sw = __builtin_amdgcn_permlane16_swap(fu(rm[r]), fu(rm[r]), false, false); rm[r] = uf(kw ? sw[1] : sw[0]); }
      res[2] += rm;
    }
    TS(8 + Lr * 12 + 8);
    const v2u xa = pk4(res[0]), xb = pk4(res[1]), xc = pk4(res[2]);
    if (!last) {
      const float sq = red_add(dot2(xa.x, dot2(xa.y, 0.f)) + dot2(xb.x, dot2(xb.y, 0.f))) + dot2(xc.x, dot2(xc.y, 0.f));
      gst128(rs, WS_FX + Lr * FRAG_BUF + To * FT + go * 1024 + l16, cat(xa, xb));
      gst128(rs, gq == 0 ? WS_FX + Lr * FRAG_BUF + To * FT + 16384 + go * 256 + tok * 16 : OOB, v4u{xc.x, xc.y, fu(sq), 0u});
#pragma unroll
      for (int i = 0; i < RING; ++i) ring[i] = wfrag(fr_slot(i));
      PIN();
      DRAIN();
      RAWBAR();
      if (tid == 448) set_flag(ws, flm + 32 + p, g, tag);
      TS(8 + Lr * 12 + 9);
      dma_range<7>(ws, nxtW, Lb, 31, 55, w, l16);
      dma_range<7>(ws, nxtW, Lb, 76, 90, w, l16);
      DRAIN();
      if (w == 0) poll(ws, poll_off(flm + 32 + p, flm + 32 + p, lane), true, tag);
      RAWBAR();
      TS(8 + Lr * 12 + 10);
    } else {
      GAS unsigned char* orow = (GAS unsigned char*)out + (16 * To + tok) * 1152 + 72 * go;
      *(GAS v2u*)(orow + 8 * gq) = xa;
      *(GAS v2u*)(orow + 40 + 8 * gq) = xb;
      if (gq == 0) *(GAS v2u*)(orow + 32) = xc;
      if (b == 0 && tid == 0) __hip_atomic_store((GAS u64*)(ws + WS_EPOCH), tag, RLX_AGENT);
      DRAIN();
      TS(8 + Lr * 12 + 9);
    }
  };
#pragma unroll 1
  for (int it = 0; it < 2; ++it) { layer(IC(0), it); layer(IC(1), it); }
}

extern "C" void kernel_launch(void* const* d_in, const int* in_sizes, int n_in, void* d_out, int out_size, void* d_ws, size_t ws_size, hipStream_t stream) {
  (void)hipFuncSetAttribute((const void*)fused_kernel, hipFuncAttributeMaxDynamicSharedMemorySize, L_TOTAL);
  fused_kernel<<<256, 512, L_TOTAL, stream>>>((const u16*)d_in[0], (const u16*)d_in[1], (const u16*)d_in[2], (const u16*)d_in[3],
                                              (const u16*)d_in[5], (const u16*)d_in[6], (u16*)d_out, (unsigned char*)d_ws);
}
```

```cpp
#include <hip/hip_runtime.h>
#include <type_traits>
#include <utility>
typedef unsigned short u16;
typedef unsigned long long u64;
typedef unsigned v4u __attribute__((ext_vector_type(4)));
typedef unsigned v2u __attribute__((ext_vector_type(2)));
typedef float f32x4 __attribute__((ext_vector_type(4)));
typedef float f32x2 __attribute__((ext_vector_type(2)));
typedef short bf16x8 __attribute__((ext_vector_type(8)));
typedef short s16x4 __attribute__((ext_vector_type(4)));
typedef __bf16 bf16x2 __attribute__((ext_vector_type(2)));
#define GAS __attribute__((address_space(1)))
#define LAS __attribute__((address_space(3)))
#define RLX_AGENT __ATOMIC_RELAXED, __HIP_MEMORY_SCOPE_AGENT
#define DEV __device__ __forceinline__
#define IC(n) std::integral_constant<int, (n)>{}

constexpr int WS_EPOCH = 0;
constexpr int WS_FLAGS = 4096;
constexpr int FL_RT1 = 0, FL_RT2 = 16, FL_HOP = 32;
constexpr int WQ_BLOB = 129024, WO_BLOB = 82944;
constexpr int WS_WQ = 1 << 20;
constexpr int WS_WO = WS_WQ + 2 * 16 * WQ_BLOB;
constexpr int FT = 20480;
constexpr int FRAG_BUF = 128 * FT;
constexpr int WS_FX = 8 << 20;
constexpr int WS_FO = WS_FX + 3 * FRAG_BUF;
constexpr int KVX_REC = 2304;
constexpr int KVX_BUF = 128 * 16 * KVX_REC;
constexpr int WS_KVX = WS_FO + 4 * FRAG_BUF;
constexpr int KVP_REC = 5760;
constexpr int WS_KVP = WS_KVX + 2 * KVX_BUF;
constexpr int WS_CSP = WS_KVP + 2 * 256 * KVP_REC;
constexpr int WS_DBG = WS_CSP + 2 * 16 * 18 * 448;
constexpr int WS_TOTAL = WS_DBG + 256 * 64 * 8 * 10;
static_assert(WS_WO + 2 * 8 * WO_BLOB <= WS_FX, "ws map");
constexpr int L_O = 77824, L_TOTAL = 163840;
constexpr int O_LATE = 50;
constexpr int L_STG = WQ_BLOB;
constexpr int L_XCH = 31 * 1024;
constexpr int IMG_REC = 2320, STG_REC = 3472;
constexpr int L_CS = L_O + WO_BLOB;
constexpr int L_PH = L_CS + 896;
constexpr int L_ONEQ = L_TOTAL - 16;
static_assert(L_PH + 1480 <= L_ONEQ && L_PH % 8 == 0 && L_O + O_LATE * 1024 == WQ_BLOB && 24 * IMG_REC <= 55 * 1024, "lds map");
constexpr int OOB = 0x7ffffff0;
constexpr unsigned SPIN_CAP = 1u << 14;
constexpr float LOG2E = 1.4426950408889634f;
constexpr float BIG = 18446744073709551616.0f;
constexpr int RING = 14;

DEV f32x4 mfma(v4u a, v4u b, f32x4 c) {
  return __builtin_amdgcn_mfma_f32_16x16x32_bf16(__builtin_bit_cast(bf16x8, a), __builtin_bit_cast(bf16x8, b), c, 0, 0, 0);
}
DEV unsigned pk2(float lo, float hi) { f32x2 v = {lo, hi}; return __builtin_bit_cast(unsigned, __builtin_convertvector(v, bf16x2)); }
DEV v2u pk4(f32x4 v) { return v2u{pk2(v.x, v.y), pk2(v.z, v.w)}; }
DEV unsigned fu(float f) { return __builtin_bit_cast(unsigned, f); }
DEV float uf(unsigned u) { return __builtin_bit_cast(float, u); }
DEV f32x4 unpk4(v2u u) { return f32x4{uf(u.x << 16), uf(u.x & 0xffff0000u), uf(u.y << 16), uf(u.y & 0xffff0000u)}; }
DEV float dot2(unsigned a, float c) { return __builtin_amdgcn_fdot2_f32_bf16(__builtin_bit_cast(bf16x2, a), __builtin_bit_cast(bf16x2, a), c, false); }
DEV v4u cat(v2u a, v2u b) { return v4u{a.x, a.y, b.x, b.y}; }
DEV float red_add(float x) { auto a = __builtin_amdgcn_permlane16_swap(fu(x), fu(x), false, false); x = uf(a[0]) + uf(a[1]); auto b = __builtin_amdgcn_permlane32_swap(fu(x), fu(x), false, false); return uf(b[0]) + uf(b[1]); }
DEV float red_max(float x) { auto a = __builtin_amdgcn_permlane16_swap(fu(x), fu(x), false, false); x = fmaxf(uf(a[0]), uf(a[1])); auto b = __builtin_amdgcn_permlane32_swap(fu(x), fu(x), false, false); return fmaxf(uf(b[0]), uf(b[1])); }
DEV float red_min(float x) { auto a = __builtin_amdgcn_permlane16_swap(fu(x), fu(x), false, false); x = fminf(uf(a[0]), uf(a[1])); auto b = __builtin_amdgcn_permlane32_swap(fu(x), fu(x), false, false); return fminf(uf(b[0]), uf(b[1])); }
DEV float odd_rows(float x) { auto a = __builtin_amdgcn_permlane16_swap(fu(x), fu(x), false, false); return uf(a[1]); }
DEV float hi_half(float x) { auto a = __builtin_amdgcn_permlane32_swap(fu(x), fu(x), false, false); return uf(a[1]); }

typedef LAS unsigned char* lptr;
DEV v4u lds128(lptr L, int off) { return *(LAS v4u*)(L + off); }
DEV v2u lds64(lptr L, int off) { return *(LAS v2u*)(L + off); }
DEV v2u ldstr(lptr L, int off) { return __builtin_bit_cast(v2u, __builtin_amdgcn_ds_read_tr16_b64_v4i16((LAS s16x4*)(L + off))); }
DEV void sts128(lptr L, int off, v4u v) { *(LAS v4u*)(L + off) = v; }
DEV void sts64(lptr L, int off, v2u v) { *(LAS v2u*)(L + off) = v; }
#define BAR() do { asm volatile("s_waitcnt lgkmcnt(0)" ::: "memory"); __builtin_amdgcn_s_barrier(); asm volatile("" ::: "memory"); } while (0)
#define RAWBAR() do { asm volatile("" ::: "memory"); __builtin_amdgcn_s_barrier(); asm volatile("" ::: "memory"); } while (0)
#define VMWAIT(n) asm volatile("s_waitcnt vmcnt(%0)" :: "n"(n) : "memory")
#define DRAIN() VMWAIT(0)
#define PIN() __builtin_amdgcn_sched_barrier(0)
#define PINM() __builtin_amdgcn_sched_barrier(0x406)
typedef __amdgpu_buffer_rsrc_t rsrc_t;
DEV v4u gld128(rsrc_t r, int off, int soff = 0) { return __builtin_amdgcn_raw_buffer_load_b128(r, off, soff, 16); }
DEV void gst128(rsrc_t r, int off, v4u v) { __builtin_amdgcn_raw_buffer_store_b128(v, r, off, 0, 16); }
DEV void gst64(rsrc_t r, int off, v2u v) { __builtin_amdgcn_raw_buffer_store_b64(v, r, off, 0, 16); }
template <int OFF> DEV void aload(v4u& d, const unsigned char* sbase, int voff) { asm volatile("global_load_dwordx4 %0, %1, %2 offset:%3 sc1" : "=v"(d) : "v"(voff), "s"(sbase), "n"(OFF) : "memory"); }
DEV void aload64(v2u& d, const unsigned char* sbase, int voff) { asm volatile("global_load_dwordx2 %0, %1, %2 sc1" : "=v"(d) : "v"(voff), "s"(sbase) : "memory"); }
DEV void aload64p(v2u& d, const void* sbase, int voff) { asm volatile("global_load_dwordx2 %0, %1, %2" : "=v"(d) : "v"(voff), "s"(sbase) : "memory"); }
#define GOT(n, reg) asm volatile("s_waitcnt vmcnt(%1)" : "+v"(reg) : "n"(n) : "memory")
DEV void dmaf(const unsigned char* ws, int blob, unsigned ldsb, int f, int l16) {
  unsigned keep, st; int vt;
  asm volatile("s_mov_b32 %0, m0\n\ts_lshl_b32 %1, %5, 10\n\ts_add_u32 m0, %1, %4\n\ts_add_u32 %1, %1, %7\n\tv_add_u32 %2, %1, %6\n\tglobal_load_lds_dwordx4 %2, %3 sc1\n\ts_mov_b32 m0, %0"
               : "=&s"(keep), "=&s"(st), "=&v"(vt) : "s"(ws), "s"(ldsb), "s"(f), "v"(l16), "s"(__builtin_amdgcn_readfirstlane(blob)) : "memory", "scc");
}
DEV void dma_in(const void* base, int voff, unsigned ldsdst) {
  unsigned keep;
  asm volatile("s_mov_b32 %0, m0\n\ts_mov_b32 m0, %3\n\ts_nop 0\n\tglobal_load_lds_dwordx4 %1, %2\n\ts_mov_b32 m0, %0" : "=&s"(keep) : "v"(voff), "s"(base), "s"(ldsdst) : "memory");
}
template <int NW> DEV void dma_range(const unsigned char* ws, int blob, unsigned ldsbase, int f0, int f1, int w, int l16) {
  if (NW == 7 && w == 0) return;
#pragma unroll 1
  for (int f = f0 + (NW == 7 ? w - 1 : w); f < f1; f += NW) dmaf(ws, blob, ldsbase, f, l16);
}
DEV int poll_off(int lineA, int lineB, int lane) { return WS_FLAGS + ((lane & 16) ? lineB : lineA) * 128 + (lane & 15) * 8; }
DEV void poll(const unsigned char* ws, int off, bool care, u64 tag) {
  for (unsigned spins = 0; spins < SPIN_CAP; ++spins) if (__all(!care || __hip_atomic_load((const GAS u64*)(ws + off), RLX_AGENT) == tag)) break;
  __builtin_amdgcn_fence(__ATOMIC_ACQUIRE, "wavefront");
}
DEV void set_flag(unsigned char* ws, int line, int idx, u64 tag) { __hip_atomic_store((GAS u64*)(ws + WS_FLAGS + line * 128) + idx, tag, RLX_AGENT); }
DEV int unit_row(int s, int i) {
  return s < 16 ? 36 * s + (i < 16 ? i : i + 4) : 36 * (8 * (s - 16) + 2 * ((i >> 2) & 3) + (i >> 4)) + 16 + (i & 3);
}
#define TS(i) do {} while (0)

template <bool SAFE> DEV bool phi_maps(const f32x4 (&pv)[2][2], int gq, v2u (&pl)[2], v2u (&ml)[2], v2u (&hq)[2]) {
  f32x4 ep[2], em[2]; float hp[2][2], hm[2][2], sp[2], sm[2];
#pragma unroll
  for (int h = 0; h < 2; ++h) {
    const f32x4 lo = pv[h][0], hi = pv[h][1];
    float mx, mn;
    if (SAFE) {
      mx = red_max(fmaxf(fmaxf(fmaxf(lo.x, lo.y), fmaxf(lo.z, lo.w)), fmaxf(hi.x, hi.y))) * LOG2E;
      mn = red_min(fminf(fminf(fminf(lo.x, lo.y), fminf(lo.z, lo.w)), fminf(hi.x, hi.y))) * LOG2E;
    } else mx = mn = hi.x * LOG2E;
#pragma unroll
    for (int r = 0; r < 4; ++r) { ep[h][r] = __builtin_amdgcn_exp2f(__builtin_fmaf(lo[r], LOG2E, -mx)); em[h][r] = __builtin_amdgcn_exp2f(-__builtin_fmaf(lo[r], LOG2E, -mn)); }
    hp[h][0] = SAFE ? __builtin_amdgcn_exp2f(__builtin_fmaf(hi.x, LOG2E, -mx)) : 1.0f;
    hm[h][0] = SAFE ? __builtin_amdgcn_exp2f(-__builtin_fmaf(hi.x, LOG2E, -mn)) : 1.0f;
    hp[h][1] = __builtin_amdgcn_exp2f(__builtin_fmaf(hi.y, LOG2E, -mx)); hm[h][1] = __builtin_amdgcn_exp2f(-__builtin_fmaf(hi.y, LOG2E, -mn));
    sp[h] = (ep[h].x + ep[h].y) + (ep[h].z + ep[h].w); sm[h] = (em[h].x + em[h].y) + (em[h].z + em[h].w);
  }
  auto a0 = __builtin_amdgcn_permlane16_swap(fu(sp[0]), fu(sm[0]), false, false);
  auto a1 = __builtin_amdgcn_permlane16_swap(fu(sp[1]), fu(sm[1]), false, false);
  auto a2 = __builtin_amdgcn_permlane32_swap(fu(uf(a0[0]) + uf(a0[1])), fu(uf(a1[0]) + uf(a1[1])), false, false);
  const float h0 = (gq & 1) ? hm[0][0] + hm[0][1] : hp[0][0] + hp[0][1], h1 = (gq & 1) ? hm[1][0] + hm[1][1] : hp[1][0] + hp[1][1];
  const float tot = (uf(a2[0]) + uf(a2[1])) + (gq < 2 ? h0 : h1);
  const float rc = __builtin_amdgcn_rcpf(tot);
  auto b0 = __builtin_amdgcn_permlane32_swap(fu(rc), fu(rc), false, false);
#pragma unroll
  for (int h = 0; h < 2; ++h) {
    auto b1 = __builtin_amdgcn_permlane16_swap(b0[h], b0[h], false, false);
    const float rp = uf(b1[0]), rm = uf(b1[1]);
    pl[h] = pk4(ep[h] * rp); ml[h] = pk4(em[h] * rm);
    hq[h] = v2u{pk2(hp[h][0] * rp, hp[h][1] * rp), pk2(hm[h][0] * rm, hm[h][1] * rm)};
  }
  return tot < BIG;
}
DEV void phi_pair(const f32x4 (&pv)[2][2], int gq, v2u (&pl)[2], v2u (&ml)[2], v2u (&hq)[2]) {
  if (__builtin_expect(!__all(phi_maps<false>(pv, gq, pl, ml, hq)), 0)) phi_maps<true>(pv, gq, pl, ml, hq);
}
constexpr int fr_s(int ty, int i) { return i >= 90 ? (i - 90) / 2 : ty == 1 ? i / 5 : i < 54 ? i / 3 : (i - 54) / 2; }
constexpr int fr_t(int ty, int i) { return i >= 90 ? 5 + (i - 90) % 2 : ty == 1 ? i % 5 : i < 54 ? (i % 3 == 2 ? 4 : i % 3) : 2 + (i - 54) % 2; }
__host__ __device__ constexpr int fr_slot(int i) { return i < 21 ? 55 + i : i < 76 ? i - 21 : i; }
constexpr int xn(int s) { return s < 0 ? 6 : s < 13 ? 7 + s + s / 2 : 25; }
constexpr int on(int u) { return u < 0 ? 6 : u < 7 ? 7 + u : 14; }
template <int lo, int hi, class F> DEV void steps(F&& f) {
  [&]<int... k>(std::integer_sequence<int, k...>) { (f(IC(lo + k)), ...); }(std::make_integer_sequence<int, hi - lo>{});
}

__global__ void __launch_bounds__(512, 2) fused_kernel(const u16* __restrict__ x0, const u16* __restrict__ wA, const u16* __restrict__ wphi, const u16* __restrict__ woA,
                                                       const u16* __restrict__ wB, const u16* __restrict__ woB, u16* __restrict__ out, unsigned char* ws0) {
  unsigned char* const ws = ws0;
  extern __shared__ __attribute__((aligned(16))) unsigned char smem[];
  const lptr L = (lptr)smem;
  const unsigned Lb = (unsigned)(uintptr_t)L;
  const int b = blockIdx.x, tid = threadIdx.x, lane0 = tid & 63, w0 = __builtin_amdgcn_readfirstlane(tid >> 6);
  const int p0 = ((b & 7) >> 1) * 4 + (b >> 6), g0 = (b & 1) * 8 + ((b >> 3) & 7);
  const rsrc_t rs = __builtin_amdgcn_make_buffer_rsrc((void*)ws, 0, WS_TOTAL, 0x00020000);
  TS(0);
  if (w0 == 7) {
    u64 pc; asm volatile("s_getpc_b64 %0" : "=s"(pc));
    const int ch = (b >> 3) * 2;
    v4u d0, d1;
    if (ch < 38) {
      asm volatile("global_load_dwordx4 %0, %2, %3\n\tglobal_load_dwordx4 %1, %2, %3 offset:1024" : "=&v"(d0), "=&v"(d1) : "v"(ch * 1024 + lane0 * 16), "s"(pc & ~15ull) : "memory");
      asm volatile("s_waitcnt vmcnt(0)" : "+v"(d0), "+v"(d1) :: "memory");
    }
  }
  u64 tag;
  v4u xf[18];
  f32x4 res[3];
  v4u ring[15];
  float ssq;
  const f32x4 z = {0.f, 0.f, 0.f, 0.f};
  auto colsums7 = [&](int type) {
    int lane = lane0;
    asm volatile("" : "+v"(lane));
    const int base = WS_CSP + (type * 16 + g0) * 18 * 448 + (lane >> 5) * 9 * 448 + (lane & 31) * 16;
    v4u c[9];
#pragma unroll
    for (int i = 0; i < 9; ++i) c[i] = gld128(rs, (lane & 31) < 28 ? base + i * 448 : OOB);
    f32x4 t = __builtin_bit_cast(f32x4, c[0]);
#pragma unroll
    for (int i = 1; i < 9; ++i) t += __builtin_bit_cast(f32x4, c[i]);
#pragma unroll
    for (int r = 0; r < 4; ++r) { auto sw = __builtin_amdgcn_permlane32_swap(fu(t[r]), fu(t[r]), false, false); t[r] = uf(sw[0]) + uf(sw[1]); }
    if (lane < 28) *(LAS f32x4*)(L + L_CS + type * 448 + lane * 16) = t;
  };
  {
    const int lane = lane0, tok = lane & 15, gq = lane >> 4, w = w0, p = p0, g = g0, T = 8 * p + w;
    const int trq = (lane >> 2) & 3, trp = lane & 3;
    constexpr int RAW_A = 147456;
    constexpr int RAW_B = WQ_BLOB, RAW_OA = RAW_B + 6912, RAW_OB = RAW_OA + 2304, RAW_E1 = RAW_OB + 2304;
    const v4u ones = {0x3F803F80u, 0x3F803F80u, 0x3F803F80u, 0x3F803F80u};
    const int pc0 = tid, pc1 = tid + 512 > 863 ? 863 : tid + 512;
    const int i0 = pc0 / 27, q0 = pc0 - i0 * 27, i1 = pc1 / 27, q1 = pc1 - i1 * 27;
    const int co0 = (q0 / 9) * 1080 + q0 * 8 + g * 72, co1 = (q1 / 9) * 1080 + q1 * 8 + g * 72;
    auto wq_load = [&](const u16* W, int s, v2u (&d)[2]) { aload64p(d[0], W, unit_row(s, i0) * 3456 + co0); aload64p(d[1], W, unit_row(s, i1) * 3456 + co1); };
    auto wq_put = [&](int img, const v2u (&d)[2]) { sts64(L, img + pc0 * 8, d[0]); sts64(L, img + pc1 * 8, d[1]); };
    const int wco = (w & 1) * 40 + (w < 2 ? 72 : w < 4 ? 144 : 0) - (w > 4 ? 40 : 0) + (w == 6 ? 80 : 0);
    auto wq_emit = [&](int img, int type, int s) {
      if (w < 7) {
        int a0 = img + (4 * gq + trq) * 216 + (w == 4 ? trp * 72 + 32 : wco + trp * 8), a1 = a0 + 16 * 216;
        if (w == 4 && trp == 3) a0 = a1 = L_ONEQ;
        const int j = w >= 5 ? 90 + 2 * s + w - 5 : type == 1 ? 5 * s + w : (w == 2 || w == 3) ? 54 + 2 * s + w - 2 : 3 * s + (w >> 1 ? 2 : w);
        const v4u f = cat(ldstr(L, a0), ldstr(L, a1));
        gst128(rs, WS_WQ + (type * 16 + g) * WQ_BLOB + fr_slot(j) * 1024 + lane * 16, f);
        gst128(rs, tok == 0 ? WS_CSP + ((type * 16 + g) * 18 + s) * 448 + w * 64 + gq * 16 : OOB, __builtin_bit_cast(v4u, mfma(f, ones, z)));
      }
    };
    const int po = tid > 287 ? 287 : tid, io = po / 9;
    auto wo_load = [&](const u16* W, int s, v2u& d) { aload64p(d, W, unit_row(s, io) * 1152 + g * 72 + (po - io * 9) * 8); };
    auto wo_emit = [&](int img, int type, int s, int t) {
      const int a0 = img + (4 * gq + trq) * 72 + (t == 2 ? 32 : t * 40 + trp * 8);
      const int idx = s < 16 ? 2 * (s & 7) + 2 + (s >> 3) : s - 16;
      const int o = WS_WO + (type * 8 + (g >> 1)) * WO_BLOB + (17 - idx) * 4608 + (t < 2 ? (g & 1) * 2048 + t * 1024 + lane * 16 : 4096 + (gq * 8 + (g & 1) * 4 + tok) * 16);
      gst128(rs, (t == 2 && tok >= 4) ? OOB : o, cat(ldstr(L, a0), ldstr(L, a0 + 16 * 72)));
    };
    v2u dA[2], dE[2], dB[2], dO[3], r0, r1, r2, ep, dP;
    const int xs = 16 + (p & 1);
    wq_load(wA, p, dA);
    wq_load(p < 2 ? wA : wB, xs, dE);
    aload64(ep, ws, WS_EPOCH);
    PIN();
    auto x_dma = [&](auto i_) {
      constexpr int i = decltype(i_)::value;
      const int ci = i * 64 + lane, r = (ci * 911) >> 16;
      const int src = T * 18432 + ci * 16 - r * 16 + (ci - r * 72 < r ? 1152 : 0);
      dma_in(x0, src, Lb + w * 18432 + i * 1024);
    };
    steps<0, 6>(x_dma);
    if (tid == 0) sts64(L, L_ONEQ, v2u{0x3F80u, 0u});
    asm volatile("s_waitcnt vmcnt(6)" : "+v"(dA[0]), "+v"(dA[1]), "+v"(dE[0]), "+v"(dE[1]), "+v"(ep) :: "memory");
    wq_put(RAW_A, dA);
    tag = ((u64)(unsigned)__builtin_amdgcn_readfirstlane(ep.x) | ((u64)(unsigned)__builtin_amdgcn_readfirstlane(ep.y) << 32)) + 1;
    BAR();
    wq_emit(RAW_A, 0, p);
    if (p < 2) { BAR(); wq_put(RAW_A, dE); BAR(); wq_emit(RAW_A, 0, xs); }
    PIN();
    steps<6, 12>(x_dma);
    VMWAIT(6);
    RAWBAR();
    if (tid == 448) set_flag(ws, FL_RT1 + g, p, tag);
    TS(1);
    steps<12, 18>(x_dma);
    wq_load(wB, p, dB); wo_load(woA, p, dO[0]); wo_load(woB, p, dO[1]); wo_load(p < 6 ? woA : woB, xs, dO[2]);
    const int xr = (16 * (8 * p + 4 * (g & 1) + (w & 3)) + tok) * 1152 + 72 * ((g & ~1) + (w >> 2));
    aload64p(r0, x0, xr + 8 * gq); aload64p(r1, x0, xr + 40 + 8 * gq); aload64p(r2, x0, xr + 32);
    const int phi_i = tid < 162 ? tid : 161;
    aload64p(dP, wphi, g * 1296 + phi_i * 8);
    VMWAIT(9);
    {
      const int xb = w * 18432 + tok * 1152;
      auto xoff = [&](int byte) { int c = (byte >> 4) + tok; c -= c >= 72 ? 72 : 0; return xb + c * 16 + (byte & 8); };
#pragma unroll
      for (int s = 0; s < 16; ++s) xf[s] = cat(lds64(L, xoff(72 * s + 8 * gq)), lds64(L, xoff(72 * s + 40 + 8 * gq)));
#pragma unroll
      for (int s = 0; s < 2; ++s) xf[16 + s] = cat(lds64(L, xoff(72 * (8 * s + 2 * gq) + 32)), lds64(L, xoff(72 * (8 * s + 2 * gq + 1) + 32)));
    }
    TS(2);
    if (w == 0) poll(ws, poll_off(FL_RT1 + g, FL_RT1 + g, lane), true, tag);
    asm volatile("s_waitcnt vmcnt(0)" : "+v"(dB[0]), "+v"(dB[1]), "+v"(dO[0]), "+v"(dO[1]), "+v"(dO[2]), "+v"(r0), "+v"(r1), "+v"(r2), "+v"(dP) :: "memory");
    BAR();
    TS(3);
    if (w == 0) colsums7(0);
    dma_range<7>(ws, WS_WQ + g * WQ_BLOB, Lb, 0, 126, w, lane * 16);
    wq_put(RAW_B, dB); sts64(L, RAW_OA + po * 8, dO[0]); sts64(L, RAW_OB + po * 8, dO[1]);
    if (p >= 2 && p < 4) wq_put(RAW_E1, dE);
    else if (p >= 4 && p < 8) sts64(L, RAW_E1 + po * 8, dO[2]);
    { const int d0 = 2 * phi_i, d1 = d0 + 1; *(LAS unsigned*)(L + L_PH + (d0 / 9) * 4 + d0 * 4) = dP.x; *(LAS unsigned*)(L + L_PH + (d1 / 9) * 4 + d1 * 4) = dP.y; }
    if (tid >= 448 && tid < 448 + 46) { const int i = tid - 448; *(LAS unsigned*)(L + L_PH + (i < 36 ? i * 40 + 36 : 1440 + (i - 36) * 4)) = 0u; }
    res[0] = unpk4(r0); res[1] = unpk4(r1); res[2] = unpk4(r2);
    {
      float q4[4] = {0.f, 0.f, 0.f, 0.f};
#pragma unroll
      for (int s = 0; s < 18; ++s)
#pragma unroll
        for (int j = 0; j < 4; ++j) q4[j] = dot2(xf[s][j], q4[j]);
      ssq = (q4[0] + q4[1]) + (q4[2] + q4[3]);
    }
    DRAIN();
    BAR();
    TS(4);
    wq_emit(RAW_B, 1, p);
    if (p >= 2 && p < 4) wq_emit(RAW_E1, 1, xs);
    if (w < 3) wo_emit(RAW_OA, 0, p, w); else if (w < 6) wo_emit(RAW_OB, 1, p, w - 3);
    if (p >= 4 && p < 8 && w < 3) wo_emit(RAW_E1, p < 6 ? 0 : 1, xs, w);
#pragma unroll
    for (int i = 0; i < RING; ++i) ring[i] = lds128(L, fr_slot(i) * 1024 + lane * 16);
    PIN();
  }

  auto layer = [&](auto ty_, const int it) __attribute__((always_inline)) {
    constexpr int ty = decltype(ty_)::value;
    const int Lr = 2 * it + ty;
    int lane = lane0, w = w0, p = p0, g = g0;
    unsigned char* ws = ws0;
    asm volatile("" : "+v"(lane), "+s"(w), "+s"(p), "+s"(g), "+s"(ws));
    const int T = 8 * p + w;
    const int tok = lane & 15, gq = lane >> 4, trq = (lane >> 2) & 3, trp = lane & 3, l16 = lane * 16;
    const bool last = (ty == 1) && (it == 1);
    int hi16 = l16 + 65536;
    asm volatile("" : "+v"(hi16));
    auto wfrag = [&](int slot) { return slot < 64 ? lds128(L, l16 + slot * 1024) : lds128(L, hi16 + (slot - 64) * 1024); };
    const int curW = WS_WQ + (ty * 16 + g) * WQ_BLOB, nxtW = WS_WQ + ((1 - ty) * 16 + g) * WQ_BLOB, curO = WS_WO + (ty * 8 + (g >> 1)) * WO_BLOB;
    const int flm = FL_HOP + Lr * 48;
    v4u rv[4];
    const int xtb = WS_FX + (Lr - 1) * FRAG_BUF + T * FT, xv0 = xtb + 4096 + l16, xv1 = xv0 + 8192, xvr = xtb + 16384 + gq * 512 + tok * 16;
    auto x_op = [&](auto k_) {
      constexpr int k = decltype(k_)::value;
      if constexpr (k < 16) aload<(k % 8) * 1024 - 4096>(xf[k], ws, k < 8 ? xv0 : xv1);
      else if constexpr (k < 20) aload<((k - 16) & 1) * 256 + ((k - 16) >> 1) * 2048>(rv[k - 16], ws, xvr);
      else dmaf(ws, curW, Lb, min(90 + (k - 20) * 8 + w, 125), l16);
    };
    if (Lr > 0) steps<0, xn(-1)>(x_op);
    PIN();
    f32x4 acc[7];
#pragma unroll
    for (int t = 0; t < 7; ++t) acc[t] = z;
    constexpr int QB = ty == 0 ? 72 : 88;
    auto qkv_step = [&](auto i_) {
      constexpr int i = decltype(i_)::value, s = fr_s(ty, i), t = fr_t(ty, i), P1 = ty == 0 ? 3 : 5;
      if (i < 16 * P1 && t == 0) { PIN(); if (Lr > 0) steps<xn(s - 1), xn(s)>(x_op); PIN(); GOT(xn(s) - s - 1, xf[s]); }
      if (i == 16 * P1) {
        PIN();
        asm volatile("s_waitcnt vmcnt(5)" : "+v"(rv[0]), "+v"(rv[1]), "+v"(rv[2]), "+v"(rv[3]) :: "memory");
        if (Lr > 0) { xf[16] = v4u{rv[0].x, rv[0].y, rv[1].x, rv[1].y}; xf[17] = v4u{rv[2].x, rv[2].y, rv[3].x, rv[3].y}; ssq = (uf(rv[0].z) + uf(rv[1].z)) + (uf(rv[2].z) + uf(rv[3].z)); }
      }
      if (i == QB) {
        DRAIN(); RAWBAR();
#pragma unroll
        for (int j = 90; j < QB + RING; ++j) ring[j % RING] = wfrag(fr_slot(j));
      }
      acc[t] = mfma(ring[i % RING], xf[s], acc[t]);
      if (i + RING < 126 && (i + RING < 90 || i >= QB)) ring[i % RING] = wfrag(fr_slot(i + RING));
      if (ty == 0 && i >= 54 && i < QB) {
        __builtin_amdgcn_sched_group_barrier(0x008, 1, 0); __builtin_amdgcn_sched_group_barrier(0x100, 1, 0);
        __builtin_amdgcn_sched_group_barrier(0x002, 6, 0);
      } else PINM();
    };
    const int csb = L_CS + ty * 448 + gq * 16;
    auto wph = [&](int h, int ft) {
      const int rl = L_PH + (20 * h + 4 * gq + trq) * 40, ru = L_PH + ((trq >> 1) == h ? (18 * h + (h ? 0 : 16) + (trq & 1)) * 40 : 1440);
      return cat(ldstr(L, rl + (ft ? 32 : trp * 8)), ldstr(L, ru + (ft ? 32 : trp * 8)));
    };
    const int vsw = gq * 128 + ((tok * 8) ^ ((gq >> 1) * 64));
    const int trl = trp * 128 + (((4 * gq + trq) * 8) ^ ((trp >> 1) * 64));
    steps<0, ty == 0 ? 54 : 90>(qkv_step);
    if (Lr == 0) {
      DRAIN();
      RAWBAR();
      if (tid == 448) set_flag(ws, FL_RT2 + g, p, tag);
    }
    const float mean = hi_half(odd_rows(acc[4].x)) * (1.0f / 576.0f);
    const float var = red_add(ssq) * (1.0f / 576.0f) - mean * mean;
    const float rstd = __builtin_amdgcn_rsqf(fmaxf(var, 0.f) + 1e-5f);
    auto fix = [&](int t, float sc) { return (acc[t] - mean * *(LAS f32x4*)(L + csb + t * 64)) * sc; };
    v2u kq[2], vq[2];
    kq[0] = pk4(fix(0, rstd)); kq[1] = pk4(fix(1, rstd));
    const f32x4 remf = fix(4, rstd);
    const v2u remq = pk4(remf);
    const int vpos = gq * 256 + ((tok * 16) ^ ((gq >> 1) * 128));
    v4u kboth, vboth;
    const int kh = w >= 3 ? 1 : 0, kft = w - 3 * kh;
    if (ty == 0) {
      const v2u ku = gq == 1 ? remq : v2u{0u, 0u};
      f32x4 pv[2][2];
#pragma unroll
      for (int h = 0; h < 2; ++h) { const v4u bk = cat(kq[h], ku); pv[h][0] = mfma(wph(h, 0), bk, z); pv[h][1] = mfma(wph(h, 1), bk, z); }
      PIN();
      v2u pl[2], ml[2], hq[2];
      const bool ok = phi_maps<false>(pv, gq, pl, ml, hq);
      steps<54, QB>(qkv_step);
      PIN();
      if (__builtin_expect(!__all(ok), 0)) phi_maps<true>(pv, gq, pl, ml, hq);
      const int sb = L_STG + w * STG_REC;
      if (gq == 2) sts64(L, sb + 3072 + tok * 8, remq);
      if (lane >= 62) sts64(L, sb + 3456 + (lane & 1) * 8, v2u{(lane & 1) ? 0u : 0x3F80u, 0u});
#pragma unroll
      for (int h = 0; h < 2; ++h) {
        sts64(L, sb + 1024 + h * 1024 + vsw, pl[h]); sts64(L, sb + 1536 + h * 1024 + vsw, ml[h]);
        if (gq == 0) sts64(L, sb + 3200 + h * 128 + tok * 8, hq[h]);
      }
      PIN();
      steps<QB, 90>(qkv_step);
      vq[0] = pk4(fix(2, rstd)); vq[1] = pk4(fix(3, rstd));
      TS(8 + Lr * 12 + 0);
      sts64(L, sb + vsw, vq[0]); sts64(L, sb + 512 + vsw, vq[1]);
      BAR();
      if (w < 6) {
        const int aa = L_STG + (kft < 2 ? 1024 + kh * 1024 + kft * 512 + trl : (trp == 0 ? 3200 + kh * 128 + (4 * gq + trq) * 8 : 3464));
        const int bl = L_STG + kh * 512 + trl, bh = L_STG + (trp == 0 ? 3072 + (4 * gq + trq) * 8 : 3456);
        v4u fa[4], fl[4], fh[4];
#pragma unroll
        for (int ks = 0; ks < 4; ++ks) {
          const int o0 = 2 * ks * STG_REC, o1 = o0 + STG_REC;
          fa[ks] = cat(ldstr(L, aa + o0), ldstr(L, aa + o1)); fl[ks] = cat(ldstr(L, bl + o0), ldstr(L, bl + o1)); fh[ks] = cat(ldstr(L, bh + o0), ldstr(L, bh + o1));
        }
        PIN();
        f32x4 dl = z, dh = z;
#pragma unroll
        for (int ks = 0; ks < 4; ++ks) { dl = mfma(fa[ks], fl[ks], dl); dh = mfma(fa[ks], fh[ks], dh); }
        const int rb = WS_KVP + ((it * 16 + g) * 16 + p) * KVP_REC + kh * 2880;
        const int slot = tok == 4 ? 2 : (tok >> 1) == kh ? (tok & 1) : -1;
        const bool rowok = kft < 2 || gq == 0;
        if (rowok) gst128(rs, rb + kft * 1024 + l16, __builtin_bit_cast(v4u, dl));
        if (rowok && slot >= 0) gst128(rs, rb + 2304 + kft * 256 + (gq * 4 + slot) * 16, __builtin_bit_cast(v4u, dh));
      } else {
#pragma unroll
        for (int j = 0; j < 4; ++j) { unsigned zz = 0u; asm volatile("" : "+v"(zz)); sts128(L, (w - 6) * 4096 + j * 1024 + l16, v4u{zz, zz, zz, zz}); }
        if (it == 0 && w == 6) { poll(ws, poll_off(FL_RT2 + g, FL_RT2 + (g ^ 1), lane), true, tag); colsums7(1); }
      }
    } else {
      const int rb = WS_KVX + it * KVX_BUF + (T * 16 + g) * KVX_REC;
      gst128(rs, rb + l16, kboth = (gq & 1) ? cat(kq[1], kq[0]) : cat(kq[0], kq[1]));
      if (gq == 1 || gq == 2) gst64(rs, rb + 2048 + (gq - 1) * 128 + tok * 8, remq);
      vq[0] = pk4(fix(2, rstd)); vq[1] = pk4(fix(3, rstd));
      TS(8 + Lr * 12 + 0);
      gst128(rs, rb + 1024 + vpos, vboth = (gq & 1) ? cat(vq[1], vq[0]) : cat(vq[0], vq[1]));
    }
    PIN();
    steps<90, 108>(qkv_step);
    DRAIN();
    RAWBAR();
    if (tid == 448) set_flag(ws, flm + g, p, tag);
    TS(8 + Lr * 12 + 1);
    if (ty == 1) {
      const int ib = (16 + w) * IMG_REC;
      sts128(L, ib + l16, kboth); sts128(L, ib + 1024 + vpos, vboth);
      if (gq == 1 || gq == 2) sts64(L, ib + 2048 + (gq - 1) * 128 + tok * 8, remq);
      if (lane < 3) { const int i = w + 8 * lane; sts128(L, i * IMG_REC + 2304, v4u{0x3F80u, 0u, (8 * (p - 2) + i >= 0) ? 0u : 0xF149u, 0u}); }
    } else dma_range<7>(ws, nxtW, Lb, 55, 76, w, l16);
    dma_range<7>(ws, curO, Lb + L_O, O_LATE, 81, w, l16);
    PIN();
    steps<108, 126>(qkv_step);
    constexpr float QS = 0.23570226039551584f * LOG2E;
    const v2u q0 = pk4(fix(5, ty == 1 ? rstd * QS : rstd)), q1 = pk4(fix(6, ty == 1 ? rstd * QS : rstd));
    v2u olo[2]; unsigned ohi[2];
    v2u pollv;
    auto poll_done = [&](bool care) {
      if (w == 0) {
        DRAIN(); PIN();
        asm volatile("" : "+v"(pollv));
        if (!__all(!care || ((u64)pollv.y << 32 | pollv.x) == tag)) poll(ws, poll_off(flm + g, flm + g, lane), care, tag);
      }
    };
    if (ty == 0) {
      v4u qf[2][2];
      {
        const v2u qu = gq == 0 ? remq : v2u{0u, 0u};
        f32x4 pv[2][2];
#pragma unroll
        for (int h = 0; h < 2; ++h) { const v4u bq = cat(h ? q1 : q0, qu); pv[h][0] = mfma(wph(h, 0), bq, z); pv[h][1] = mfma(wph(h, 1), bq, z); }
        PIN();
        if (w == 0) aload64(pollv, ws, poll_off(flm + g, flm + g, lane));
        PIN();
        v2u pl[2], ml[2], hq[2];
        phi_pair(pv, gq, pl, ml, hq);
#pragma unroll
        for (int h = 0; h < 2; ++h) { qf[h][0] = cat(pl[h], ml[h]); qf[h][1] = cat(hq[h], v2u{0u, 0u}); }
      }
      PIN();
      TS(8 + Lr * 12 + 2);
      poll_done(true);
      RAWBAR();
      TS(8 + Lr * 12 + 3);
      const int pi = w * 64 + lane;
      v4u dp[16];
      if (w < 6) {
        const int po = pi < 360 ? WS_KVP + (it * 16 + g) * 16 * KVP_REC + pi * 16 : OOB;
#pragma unroll
        for (int q = 0; q < 16; ++q) dp[q] = gld128(rs, po, q * KVP_REC);
        PIN();
        f32x4 s0 = __builtin_bit_cast(f32x4, dp[0]), s1 = __builtin_bit_cast(f32x4, dp[1]);
#pragma unroll
        for (int q = 2; q < 16; q += 2) { s0 += __builtin_bit_cast(f32x4, dp[q]); s1 += __builtin_bit_cast(f32x4, dp[q + 1]); }
        const v2u sv = pk4(s0 + s1);
        const int h = pi >= 180 ? 1 : 0, j = pi - 180 * h;
        const int e = j - 144, sl = e & 3, half = j < 144 ? (j >> 6) & 1 : (e >> 4) & 1;
        const int blk = j < 128 ? 0 : j < 144 ? 1024 : e < 32 ? 2048 : 3072;
        const int ln = j < 144 ? (j & 63) : ((e >> 2) & 3) * 16 + (sl < 2 ? 2 * h + sl : 4);
        const int dst = h * 4096 + blk + ln * 16 + half * 8;
        if (pi < 360 && (j < 144 || sl < 3)) sts64(L, dst, sv);
        if (pi < 360 && j >= 144 && sl == 2) sts64(L, dst + 128, sv);
      }
      BAR();
      TS(8 + Lr * 12 + 4);
      v4u kf[2][4];
#pragma unroll
      for (int h = 0; h < 2; ++h)
#pragma unroll
        for (int j = 0; j < 4; ++j) kf[h][j] = lds128(L, h * 4096 + j * 1024 + l16);
      PIN();
#pragma unroll
      for (int h = 0; h < 2; ++h) {
        const f32x4 dl2 = mfma(kf[h][1], qf[h][1], mfma(kf[h][0], qf[h][0], z));
        const f32x4 dh2 = mfma(kf[h][3], qf[h][1], mfma(kf[h][2], qf[h][0], z));
        const float r = __builtin_amdgcn_rcpf(odd_rows(dh2.x) + 1e-6f);
        olo[h] = pk4(dl2 * r); ohi[h] = h == 0 ? pk2(dh2.x * r, dh2.y * r) : pk2(dh2.z * r, dh2.w * r);
      }
    } else {
      v4u qf[2];
      {
        const v2u qr = pk4(remf * QS);
        const unsigned bias1 = gq == 1 ? 0x3F80u : 0u;
        qf[0] = cat(q0, v2u{gq == 0 ? qr.x : bias1, 0u});
        qf[1] = cat(q1, v2u{bias1, gq == 0 ? qr.y : 0u});
      }
      const int wb = w * IMG_REC;
      const int kuo = wb + (gq == 1 ? 2312 : 2048 + tok * 8);
      auto kfrag = [&](int h, int i) { return cat(lds64(L, wb + ((h ^ (gq & 1)) * 8) + l16 + i * IMG_REC), lds64(L, kuo + i * IMG_REC)); };
      PIN();
      if (w == 0) aload64(pollv, ws, poll_off(flm + g, flm + g, lane));
      PIN();
      float nm[2];
#pragma unroll
      for (int h = 0; h < 2; ++h) {
        const f32x4 sd = mfma(cat(kq[h], gq == 1 ? v2u{0u, 0u} : lds64(L, kuo + 16 * IMG_REC)), qf[h], z);
        float m = (4 * gq <= tok) ? sd.x : -3e38f;
#pragma unroll
        for (int r = 1; r < 4; ++r) m = fmaxf(m, (4 * gq + r <= tok) ? sd[r] : -3e38f);
        nm[h] = -red_max(m);
      }
      const int vl = wb + 1024 + trp * 256 + (((4 * gq + trq) * 16) ^ ((trp >> 1) * 128));
      const int vh = wb + (trp == 0 ? 2176 + (4 * gq + trq) * 8 : 2304);
      const unsigned m0 = (4 * gq + 0 <= tok ? 0xffffu : 0u) | (4 * gq + 1 <= tok ? 0xffff0000u : 0u);
      const unsigned m1 = (4 * gq + 2 <= tok ? 0xffffu : 0u) | (4 * gq + 3 <= tok ? 0xffff0000u : 0u);
      PIN();
      TS(8 + Lr * 12 + 2);
      poll_done(tok == p - 1 || tok == p - 2);
      RAWBAR();
      TS(8 + Lr * 12 + 3);
#pragma unroll
      for (int j = 0; j < 2; ++j) {
        const int Tg = 8 * (p - 2) + w + 8 * j, ib = (w + 8 * j) * IMG_REC;
        if (Tg >= 0) {
          const int rb = WS_KVX + it * KVX_BUF + (Tg * 16 + g) * KVX_REC;
          dmaf(ws, rb, Lb + ib, 0, l16); dmaf(ws, rb, Lb + ib, 1, l16);
          if (lane < 16) dmaf(ws, rb, Lb + ib, 2, l16);
        } else {
          unsigned zz = 0u; asm volatile("" : "+v"(zz));
          sts128(L, ib + l16, v4u{zz, zz, zz, zz}); sts128(L, ib + 1024 + l16, v4u{zz, zz, zz, zz});
          if (lane < 16) sts128(L, ib + 2048 + l16, v4u{zz, zz, zz, zz});
        }
      }
      DRAIN();
      BAR();
      TS(8 + Lr * 12 + 4);
#pragma unroll 1
      for (int pass = 0; pass < 2; ++pass) {
        v4u kf[34]; f32x4 sc[2][17]; v4u va, vb, van, vbn; f32x4 dl, dh;
        bool ok = true;
        constexpr int ord[17] = {16, 0, 1, 2, 3, 4, 5, 6, 7, 8, 9, 10, 11, 12, 13, 14, 15};
#pragma unroll
        for (int j = 0; j < 8; ++j) kf[j] = kfrag(0, ord[j]);
        auto vread = [&](int h, int ks) {
          const int i0 = ks == 0 ? 0 : 2 * ks - 1, i1 = ks == 0 ? 16 : ks < 8 ? 2 * ks : 15, vo = vl + ((h ^ (trp & 1)) * 8);
          van = cat(ldstr(L, vo + i0 * IMG_REC), ldstr(L, vo + i1 * IMG_REC));
          vbn = cat(ldstr(L, vh + i0 * IMG_REC), ldstr(L, vh + i1 * IMG_REC));
        };
        auto qk = [&](int j) { const int h = j / 17; sc[h][ord[j % 17]] = mfma(kf[j], qf[h], f32x4{nm[h], nm[h], nm[h], nm[h]}); };
        auto pexp = [&](f32x4 x) {
          f32x4 e;
#pragma unroll
          for (int r = 0; r < 4; ++r) e[r] = __builtin_amdgcn_exp2f(x[r]);
          return pk4(e);
        };
        vread(0, 0);
        PIN();
#pragma unroll
        for (int j = 0; j < 4; ++j) qk(j);
        PIN();
        auto att_step = [&](auto n_) {
          constexpr int n = decltype(n_)::value, h = n / 9, ks = n % 9;
          va = van; vb = vbn;
          if (n < 17) vread((n + 1) / 9, (n + 1) % 9);
          if (2 * n + 8 < 34) kf[2 * n + 8] = kfrag((2 * n + 8) / 17, ord[(2 * n + 8) % 17]);
          if (2 * n + 9 < 34) kf[2 * n + 9] = kfrag((2 * n + 9) / 17, ord[(2 * n + 9) % 17]);
          if (2 * n + 4 < 34) qk(2 * n + 4);
          if (2 * n + 5 < 34) qk(2 * n + 5);
          v4u bp;
          if (ks == 0) {
            f32x4 mg;
#pragma unroll
            for (int r = 0; r < 4; ++r) mg[r] = (4 * gq + r <= tok) ? sc[h][16][r] : sc[h][0][r];
            const v2u p0 = pexp(mg);
            bp = v4u{p0.x & ~m0, p0.y & ~m1, p0.x & m0, p0.y & m1};
          } else bp = cat(pexp(sc[h][2 * ks - 1]), ks < 8 ? pexp(sc[h][ks < 8 ? 2 * ks : 0]) : v2u{0u, 0u});
          dl = mfma(va, bp, ks == 0 ? z : dl); dh = mfma(vb, bp, ks == 0 ? z : dh);
          int wo_ = w; asm volatile("" : "+s"(wo_));
          if (n < 7) dmaf(ws, curO, Lb + L_O, min(n * 8 + wo_, O_LATE - 1), l16);
          else if (n < 10 && !last) dmaf(ws, nxtW, Lb, min(55 + (n - 7) * 8 + wo_, 75), l16);
          if (ks == 8) {
            const float l = odd_rows(dh.x);
            ok = ok && l < BIG;
            const float r = __builtin_amdgcn_rcpf(l);
            olo[h] = pk4(dl * r); ohi[h] = h == 0 ? pk2(dh.x * r, dh.y * r) : pk2(dh.z * r, dh.w * r);
          }
          PIN();
        };
        steps<0, 18>(att_step);
        if (__builtin_expect(__all(ok), 1)) break;
#pragma unroll
        for (int h = 0; h < 2; ++h) {
          const f32x4 s0 = mfma(kfrag(h, 0), qf[h], z), s16 = mfma(kfrag(h, 16), qf[h], z);
          float m = -3e38f;
#pragma unroll
          for (int r = 0; r < 4; ++r) m = fmaxf(m, (4 * gq + r <= tok) ? s16[r] : s0[r]);
#pragma unroll 1
          for (int i = 1; i < 16; ++i) { const f32x4 s = mfma(kfrag(h, i), qf[h], z); m = fmaxf(fmaxf(fmaxf(m, s.x), fmaxf(s.y, s.z)), s.w); }
          nm[h] = -red_max(m);
        }
      }
    }
    TS(8 + Lr * 12 + 5);
    gst128(rs, WS_FO + Lr * FRAG_BUF + T * FT + g * 1024 + l16, cat(olo[0], olo[1]));
    gst128(rs, gq == 0 ? WS_FO + Lr * FRAG_BUF + T * FT + 16384 + g * 256 + tok * 16 : OOB, v4u{ohi[0], ohi[1], 0u, 0u});
    const int kw = w >> 2, To = 8 * p + 4 * (g & 1) + (w & 3), go = (g & ~1) + kw;
    int omine = L_O + (1 - kw) * 4608 + kw * 2048 + l16, oother = L_O + (1 - kw) * 4608 + (1 - kw) * 2048 + l16, orem = L_O + (1 - kw) * 4608 + 4096 + (gq * 8 + (tok & 7)) * 16;
    asm volatile("" : "+v"(omine), "+v"(oother), "+v"(orem));
    auto ofrag = [&](int i) { const int u = i / 5, t = i % 5; return lds128(L, (8 - u) * 9216 + (t < 2 ? omine + t * 1024 : t < 4 ? oother + (t - 2) * 1024 : orem)); };
#pragma unroll
    for (int i = 0; i < 15; ++i) ring[i] = ofrag(i);
    PIN();
    DRAIN();
    RAWBAR();
    if (tid == 448) set_flag(ws, flm + 16 + p, g, tag);
    TS(8 + Lr * 12 + 6);
    if (ty == 0) { dma_range<7>(ws, curO, Lb + L_O, 0, O_LATE, w, l16); DRAIN(); }
    if (w == 0) poll(ws, poll_off(flm + 16 + p, flm + 16 + p, lane), true, tag);
    RAWBAR();
    TS(8 + Lr * 12 + 7);
    const int ob = WS_FO + Lr * FRAG_BUF + To * FT, ov0 = ob + kw * 8192 + 4096 + l16, ovr = ob + 16384 + kw * 2048 + gq * 512 + tok * 16;
    auto o_op = [&](auto k_) {
      constexpr int k = decltype(k_)::value;
      if constexpr (k < 2) aload<k * 256>(rv[k], ws, ovr);
      else if constexpr (k < 10) aload<(k - 2) * 1024 - 4096>(xf[k - 1], ws, ov0);
      else dmaf(ws, nxtW, Lb, min((k - 10) * 8 + w, 30), l16);
    };
    steps<0, on(-1)>(o_op);
    PIN();
    f32x4 oa[5] = {res[0], res[1], z, z, z};
    auto wo_step = [&](auto i_) {
      constexpr int i = decltype(i_)::value, u = i / 5, t = i % 5;
      if (t == 0) {
        PIN(); steps<on(u - 1), on(u)>(o_op); PIN();
        if (u == 0) {
          asm volatile("s_waitcnt vmcnt(%2)" : "+v"(rv[0]), "+v"(rv[1]) : "n"(on(0) - 2) : "memory");
          xf[0] = v4u{rv[0].x, rv[0].y, rv[1].x, rv[1].y};
        } else GOT(on(u) - u - 2, xf[u]);
      }
      oa[t] = mfma(ring[i % 15], xf[u], oa[t]);
      if (i + 15 < 45) ring[i % 15] = ofrag(i + 15);
      PINM();
    };
    steps<0, 45>(wo_step);
    {
      const int xm = L_XCH + w * 3072 + l16, xo = L_XCH + (w ^ 4) * 3072 + l16;
      sts128(L, xm, __builtin_bit_cast(v4u, oa[2])); sts128(L, xm + 1024, __builtin_bit_cast(v4u, oa[3])); sts128(L, xm + 2048, __builtin_bit_cast(v4u, oa[4]));
      BAR();
      res[0] = oa[0] + __builtin_bit_cast(f32x4, lds128(L, xo)); res[1] = oa[1] + __builtin_bit_cast(f32x4, lds128(L, xo + 1024));
      f32x4 rm = oa[4] + __builtin_bit_cast(f32x4, lds128(L, xo + 2048));
#pragma unroll
      for (int r = 0; r < 4; ++r) { auto sw = __builtin_amdgcn_permlane16_swap(fu(rm[r]), fu(rm[r]), false, false); rm[r] = uf(kw ? sw[1] : sw[0]); }
      res[2] += rm;
    }
    TS(8 + Lr * 12 + 8);
    const v2u xa = pk4(res[0]), xb = pk4(res[1]), xc = pk4(res[2]);
    if (!last) {
      const float sq = red_add(dot2(xa.x, dot2(xa.y, 0.f)) + dot2(xb.x, dot2(xb.y, 0.f))) + dot2(xc.x, dot2(xc.y, 0.f));
      gst128(rs, WS_FX + Lr * FRAG_BUF + To * FT + go * 1024 + l16, cat(xa, xb));
      gst128(rs, gq == 0 ? WS_FX + Lr * FRAG_BUF + To * FT + 16384 + go * 256 + tok * 16 : OOB, v4u{xc.x, xc.y, fu(sq), 0u});
#pragma unroll
      for (int i = 0; i < RING; ++i) ring[i] = wfrag(fr_slot(i));
      PIN();
      DRAIN();
      RAWBAR();
      if (tid == 448) set_flag(ws, flm + 32 + p, g, tag);
      TS(8 + Lr * 12 + 9);
      dma_range<7>(ws, nxtW, Lb, 31, 55, w, l16);
      dma_range<7>(ws, nxtW, Lb, 76, 90, w, l16);
      DRAIN();
      if (w == 0) poll(ws, poll_off(flm + 32 + p, flm + 32 + p, lane), true, tag);
      RAWBAR();
      TS(8 + Lr * 12 + 10);
    } else {
      GAS unsigned char* orow = (GAS unsigned char*)out + (16 * To + tok) * 1152 + 72 * go;
      *(GAS v2u*)(orow + 8 * gq) = xa;
      *(GAS v2u*)(orow + 40 + 8 * gq) = xb;
      if (gq == 0) *(GAS v2u*)(orow + 32) = xc;
      if (b == 0 && tid == 0) __hip_atomic_store((GAS u64*)(ws + WS_EPOCH), tag, RLX_AGENT);
      DRAIN();
      TS(8 + Lr * 12 + 9);
    }
  };
#pragma unroll 1
  for (int it = 0; it < 2; ++it) { layer(IC(0), it); layer(IC(1), it); }
}

extern "C" void kernel_launch(void* const* d_in, const int* in_sizes, int n_in, void* d_out, int out_size, void* d_ws, size_t ws_size, hipStream_t stream) {
  (void)hipFuncSetAttribute((const void*)fused_kernel, hipFuncAttributeMaxDynamicSharedMemorySize, L_TOTAL);
  fused_kernel<<<256, 512, L_TOTAL, stream>>>((const u16*)d_in[0], (const u16*)d_in[1], (const u16*)d_in[2], (const u16*)d_in[3],
                                              (const u16*)d_in[5], (const u16*)d_in[6], (u16*)d_out, (unsigned char*)d_ws);
}
```

```cpp
#include <hip/hip_runtime.h>
#include <type_traits>
#include <utility>
typedef unsigned short u16;
typedef unsigned long long u64;
typedef unsigned v4u __attribute__((ext_vector_type(4)));
typedef unsigned v2u __attribute__((ext_vector_type(2)));
typedef float f32x4 __attribute__((ext_vector_type(4)));
typedef float f32x2 __attribute__((ext_vector_type(2)));
typedef short bf16x8 __attribute__((ext_vector_type(8)));
typedef short s16x4 __attribute__((ext_vector_type(4)));
typedef __bf16 bf16x2 __attribute__((ext_vector_type(2)));
#define GAS __attribute__((address_space(1)))
#define LAS __attribute__((address_space(3)))
#define RLX_AGENT __ATOMIC_RELAXED, __HIP_MEMORY_SCOPE_AGENT
#define DEV __device__ __forceinline__
#define IC(n) std::integral_constant<int, (n)>{}

constexpr int WS_EPOCH = 0;
constexpr int WS_FLAGS = 4096;
constexpr int FL_RT1 = 0, FL_HOP = 16;
constexpr int WQ_BLOB = 129024, WO_BLOB = 82944;
constexpr int WS_WQ = 1 << 20;
constexpr int WS_WO = WS_WQ + 2 * 16 * WQ_BLOB;
constexpr int FT = 20480;
constexpr int FRAG_BUF = 128 * FT;
constexpr int WS_FX = 8 << 20;
constexpr int WS_FO = WS_FX + 3 * FRAG_BUF;
constexpr int KVX_REC = 2304;
constexpr int KVX_BUF = 128 * 16 * KVX_REC;
constexpr int WS_KVX = WS_FO + 4 * FRAG_BUF;
constexpr int KVP_REC = 5760;
constexpr int WS_KVP = WS_KVX + 2 * KVX_BUF;
constexpr int WS_CSP = WS_KVP + 2 * 256 * KVP_REC;
constexpr int WS_DBG = WS_CSP + 2 * 16 * 18 * 448;
constexpr int WS_TOTAL = WS_DBG + 256 * 64 * 8 * 10;
static_assert(WS_WO + 2 * 8 * WO_BLOB <= WS_FX, "ws map");
constexpr int L_O = 77824, L_TOTAL = 163840;
constexpr int O_LATE = 50;
constexpr int L_STG = WQ_BLOB;
constexpr int L_XCH = 31 * 1024;
constexpr int IMG_REC = 2320, STG_REC = 3472;
constexpr int L_CS = L_O + WO_BLOB;
constexpr int L_PH = L_CS + 896;
constexpr int L_ONEQ = L_TOTAL - 16;
static_assert(L_PH + 1480 <= L_ONEQ && L_PH % 8 == 0 && L_O + O_LATE * 1024 == WQ_BLOB && 24 * IMG_REC <= 55 * 1024, "lds map");
constexpr int OOB = 0x7ffffff0;
constexpr unsigned SPIN_CAP = 1u << 14;
constexpr float LOG2E = 1.4426950408889634f;
constexpr float BIG = 18446744073709551616.0f;
constexpr int RING = 14;

DEV f32x4 mfma(v4u a, v4u b, f32x4 c) {
  return __builtin_amdgcn_mfma_f32_16x16x32_bf16(__builtin_bit_cast(bf16x8, a), __builtin_bit_cast(bf16x8, b), c, 0, 0, 0);
}
DEV unsigned pk2(float lo, float hi) { f32x2 v = {lo, hi}; return __builtin_bit_cast(unsigned, __builtin_convertvector(v, bf16x2)); }
DEV v2u pk4(f32x4 v) { return v2u{pk2(v.x, v.y), pk2(v.z, v.w)}; }
DEV unsigned fu(float f) { return __builtin_bit_cast(unsigned, f); }
DEV float uf(unsigned u) { return __builtin_bit_cast(float, u); }
DEV f32x4 unpk4(v2u u) { return f32x4{uf(u.x << 16), uf(u.x & 0xffff0000u), uf(u.y << 16), uf(u.y & 0xffff0000u)}; }
DEV float dot2(unsigned a, float c) { return __builtin_amdgcn_fdot2_f32_bf16(__builtin_bit_cast(bf16x2, a), __builtin_bit_cast(bf16x2, a), c, false); }
DEV v4u cat(v2u a, v2u b) { return v4u{a.x, a.y, b.x, b.y}; }
DEV float red_add(float x) { auto a = __builtin_amdgcn_permlane16_swap(fu(x), fu(x), false, false); x = uf(a[0]) + uf(a[1]); auto b = __builtin_amdgcn_permlane32_swap(fu(x), fu(x), false, false); return uf(b[0]) + uf(b[1]); }
DEV float red_max(float x) { auto a = __builtin_amdgcn_permlane16_swap(fu(x), fu(x), false, false); x = fmaxf(uf(a[0]), uf(a[1])); auto b = __builtin_amdgcn_permlane32_swap(fu(x), fu(x), false, false); return fmaxf(uf(b[0]), uf(b[1])); }
DEV float red_min(float x) { auto a = __builtin_amdgcn_permlane16_swap(fu(x), fu(x), false, false); x = fminf(uf(a[0]), uf(a[1])); auto b = __builtin_amdgcn_permlane32_swap(fu(x), fu(x), false, false); return fminf(uf(b[0]), uf(b[1])); }
DEV float odd_rows(float x) { auto a = __builtin_amdgcn_permlane16_swap(fu(x), fu(x), false, false); return uf(a[1]); }
DEV float hi_half(float x) { auto a = __builtin_amdgcn_permlane32_swap(fu(x), fu(x), false, false); return uf(a[1]); }

typedef LAS unsigned char* lptr;
DEV v4u lds128(lptr L, int off) { return *(LAS v4u*)(L + off); }
DEV v2u lds64(lptr L, int off) { return *(LAS v2u*)(L + off); }
DEV v2u ldstr(lptr L, int off) { return __builtin_bit_cast(v2u, __builtin_amdgcn_ds_read_tr16_b64_v4i16((LAS s16x4*)(L + off))); }
DEV void sts128(lptr L, int off, v4u v) { *(LAS v4u*)(L + off) = v; }
DEV void sts64(lptr L, int off, v2u v) { *(LAS v2u*)(L + off) = v; }
#define BAR() do { asm volatile("s_waitcnt lgkmcnt(0)" ::: "memory"); __builtin_amdgcn_s_barrier(); asm volatile("" ::: "memory"); } while (0)
#define RAWBAR() do { asm volatile("" ::: "memory"); __builtin_amdgcn_s_barrier(); asm volatile("" ::: "memory"); } while (0)
#define VMWAIT(n) asm volatile("s_waitcnt vmcnt(%0)" :: "n"(n) : "memory")
#define DRAIN() VMWAIT(0)
#define PIN() __builtin_amdgcn_sched_barrier(0)
#define PINM() __builtin_amdgcn_sched_barrier(0x406)
typedef __amdgpu_buffer_rsrc_t rsrc_t;
DEV v4u gld128(rsrc_t r, int off, int soff = 0) { return __builtin_amdgcn_raw_buffer_load_b128(r, off, soff, 16); }
DEV void gst128(rsrc_t r, int off, v4u v) { __builtin_amdgcn_raw_buffer_store_b128(v, r, off, 0, 16); }
DEV void gst64(rsrc_t r, int off, v2u v) { __builtin_amdgcn_raw_buffer_store_b64(v, r, off, 0, 16); }
template <int OFF> DEV void aload(v4u& d, const unsigned char* sbase, int voff) { asm volatile("global_load_dwordx4 %0, %1, %2 offset:%3 sc1" : "=v"(d) : "v"(voff), "s"(sbase), "n"(OFF) : "memory"); }
DEV void aload64(v2u& d, const unsigned char* sbase, int voff) { asm volatile("global_load_dwordx2 %0, %1, %2 sc1" : "=v"(d) : "v"(voff), "s"(sbase) : "memory"); }
DEV void aload64p(v2u& d, const void* sbase, int voff) { asm volatile("global_load_dwordx2 %0, %1, %2" : "=v"(d) : "v"(voff), "s"(sbase) : "memory"); }
#define GOT(n, reg) asm volatile("s_waitcnt vmcnt(%1)" : "+v"(reg) : "n"(n) : "memory")
DEV void dmaf(const unsigned char* ws, int blob, unsigned ldsb, int f, int l16) {
  unsigned keep, st; int vt;
  asm volatile("s_mov_b32 %0, m0\n\ts_lshl_b32 %1, %5, 10\n\ts_add_u32 m0, %1, %4\n\ts_add_u32 %1, %1, %7\n\tv_add_u32 %2, %1, %6\n\tglobal_load_lds_dwordx4 %2, %3 sc1\n\ts_mov_b32 m0, %0"
               : "=&s"(keep), "=&s"(st), "=&v"(vt) : "s"(ws), "s"(ldsb), "s"(__builtin_amdgcn_readfirstlane(f)), "v"(l16), "s"(__builtin_amdgcn_readfirstlane(blob)) : "memory", "scc");
}
DEV void dma_in(const void* base, int voff, unsigned ldsdst) {
  unsigned keep;
  asm volatile("s_mov_b32 %0, m0\n\ts_mov_b32 m0, %3\n\ts_nop 0\n\tglobal_load_lds_dwordx4 %1, %2\n\ts_mov_b32 m0, %0" : "=&s"(keep) : "v"(voff), "s"(base), "s"(ldsdst) : "memory");
}
template <int NW> DEV void dma_range(const unsigned char* ws, int blob, unsigned ldsbase, int f0, int f1, int w, int l16) {
  if (NW == 7 && w == 0) return;
#pragma unroll 1
  for (int f = f0 + (NW == 7 ? w - 1 : w); f < f1; f += NW) dmaf(ws, blob, ldsbase, f, l16);
}
DEV int poll_off(int lineA, int lineB, int lane) { return WS_FLAGS + ((lane & 16) ? lineB : lineA) * 128 + (lane & 15) * 8; }
DEV void poll(const unsigned char* ws, int off, bool care, u64 tag) {
  for (unsigned spins = 0; spins < SPIN_CAP; ++spins) if (__all(!care || __hip_atomic_load((const GAS u64*)(ws + off), RLX_AGENT) == tag)) break;
  __builtin_amdgcn_fence(__ATOMIC_ACQUIRE, "wavefront");
}
DEV void set_flag(unsigned char* ws, int line, int idx, u64 tag) { __hip_atomic_store((GAS u64*)(ws + WS_FLAGS + line * 128) + idx, tag, RLX_AGENT); }
DEV int unit_row(int s, int i) {
  return s < 16 ? 36 * s + (i < 16 ? i : i + 4) : 36 * (8 * (s - 16) + 2 * ((i >> 2) & 3) + (i >> 4)) + 16 + (i & 3);
}
#define TS(i) do {} while (0)

template <bool SAFE> DEV bool phi_maps(const f32x4 (&pv)[2][2], int gq, v2u (&pl)[2], v2u (&ml)[2], v2u (&hq)[2]) {
  f32x4 ep[2], em[2]; float hp[2][2], hm[2][2], sp[2], sm[2];
#pragma unroll
  for (int h = 0; h < 2; ++h) {
    const f32x4 lo = pv[h][0], hi = pv[h][1];
    float mx, mn;
    if (SAFE) {
      mx = red_max(fmaxf(fmaxf(fmaxf(lo.x, lo.y), fmaxf(lo.z, lo.w)), fmaxf(hi.x, hi.y))) * LOG2E;
      mn = red_min(fminf(fminf(fminf(lo.x, lo.y), fminf(lo.z, lo.w)), fminf(hi.x, hi.y))) * LOG2E;
    } else mx = mn = hi.x * LOG2E;
#pragma unroll
    for (int r = 0; r < 4; ++r) { ep[h][r] = __builtin_amdgcn_exp2f(__builtin_fmaf(lo[r], LOG2E, -mx)); em[h][r] = __builtin_amdgcn_exp2f(-__builtin_fmaf(lo[r], LOG2E, -mn)); }
    hp[h][0] = SAFE ? __builtin_amdgcn_exp2f(__builtin_fmaf(hi.x, LOG2E, -mx)) : 1.0f;
    hm[h][0] = SAFE ? __builtin_amdgcn_exp2f(-__builtin_fmaf(hi.x, LOG2E, -mn)) : 1.0f;
    hp[h][1] = __builtin_amdgcn_exp2f(__builtin_fmaf(hi.y, LOG2E, -mx)); hm[h][1] = __builtin_amdgcn_exp2f(-__builtin_fmaf(hi.y, LOG2E, -mn));
    sp[h] = (ep[h].x + ep[h].y) + (ep[h].z + ep[h].w); sm[h] = (em[h].x + em[h].y) + (em[h].z + em[h].w);
  }
  auto a0 = __builtin_amdgcn_permlane16_swap(fu(sp[0]), fu(sm[0]), false, false);
  auto a1 = __builtin_amdgcn_permlane16_swap(fu(sp[1]), fu(sm[1]), false, false);
  auto a2 = __builtin_amdgcn_permlane32_swap(fu(uf(a0[0]) + uf(a0[1])), fu(uf(a1[0]) + uf(a1[1])), false, false);
  const float h0 = (gq & 1) ? hm[0][0] + hm[0][1] : hp[0][0] + hp[0][1], h1 = (gq & 1) ? hm[1][0] + hm[1][1] : hp[1][0] + hp[1][1];
  const float tot = (uf(a2[0]) + uf(a2[1])) + (gq < 2 ? h0 : h1);
  const float rc = __builtin_amdgcn_rcpf(tot);
  auto b0 = __builtin_amdgcn_permlane32_swap(fu(rc), fu(rc), false, false);
#pragma unroll
  for (int h = 0; h < 2; ++h) {
    auto b1 = __builtin_amdgcn_permlane16_swap(b0[h], b0[h], false, false);
    const float rp = uf(b1[0]), rm = uf(b1[1]);
    pl[h] = pk4(ep[h] * rp); ml[h] = pk4(em[h] * rm);
    hq[h] = v2u{pk2(hp[h][0] * rp, hp[h][1] * rp), pk2(hm[h][0] * rm, hm[h][1] * rm)};
  }
  return tot < BIG;
}
DEV void phi_pair(const f32x4 (&pv)[2][2], int gq, v2u (&pl)[2], v2u (&ml)[2], v2u (&hq)[2]) {
  if (__builtin_expect(!__all(phi_maps<false>(pv, gq, pl, ml, hq)), 0)) phi_maps<true>(pv, gq, pl, ml, hq);
}
constexpr int fr_s(int i) { return i >= 90 ? (i - 90) / 2 : i / 5; }
constexpr int fr_t(int i) { return i >= 90 ? 5 + (i - 90) % 2 : i % 5; }
__host__ __device__ constexpr int fr_slot(int i) { return i < 21 ? 55 + i : i < 76 ? i - 21 : i; }
constexpr int xn(int s) { return s < 0 ? 3 : s < 10 ? 5 + 2 * s : 25; }
constexpr int on(int ty, int u) { return u < 0 ? 4 : ty == 1 ? (u < 4 ? 6 + 2 * u : 14) : (u < 6 ? 6 + 2 * u : 17); }
template <int lo, int hi, class F> DEV void steps(F&& f) {
  [&]<int... k>(std::integer_sequence<int, k...>) { (f(IC(lo + k)), ...); }(std::make_integer_sequence<int, hi - lo>{});
}

__global__ void __launch_bounds__(512, 2) fused_kernel(const u16* __restrict__ x0, const u16* __restrict__ wA, const u16* __restrict__ wphi, const u16* __restrict__ woA,
                                                       const u16* __restrict__ wB, const u16* __restrict__ woB, u16* __restrict__ out, unsigned char* ws0) {
  unsigned char* const ws = ws0;
  extern __shared__ __attribute__((aligned(16))) unsigned char smem[];
  const lptr L = (lptr)smem;
  const unsigned Lb = (unsigned)(uintptr_t)L;
  const int b = blockIdx.x, tid = threadIdx.x, lane0 = tid & 63, w0 = __builtin_amdgcn_readfirstlane(tid >> 6);
  const int p0 = ((b & 7) >> 1) * 4 + (b >> 6), g0 = (b & 1) * 8 + ((b >> 3) & 7);
  const rsrc_t rs = __builtin_amdgcn_make_buffer_rsrc((void*)ws, 0, WS_TOTAL, 0x00020000);
  TS(0);
  u64 tag;
  v4u xf[18];
  f32x4 res[3];
  v4u ring[15];
  float ssq;
  const f32x4 z = {0.f, 0.f, 0.f, 0.f};
  auto colsums7 = [&](int type) {
    int lane = lane0;
    asm volatile("" : "+v"(lane));
    const int base = WS_CSP + (type * 16 + g0) * 18 * 448 + (lane >> 5) * 9 * 448 + (lane & 31) * 16;
    v4u c[9];
#pragma unroll
    for (int i = 0; i < 9; ++i) c[i] = gld128(rs, (lane & 31) < 28 ? base + i * 448 : OOB);
    f32x4 t = __builtin_bit_cast(f32x4, c[0]);
#pragma unroll
    for (int i = 1; i < 9; ++i) t += __builtin_bit_cast(f32x4, c[i]);
#pragma unroll
    for (int r = 0; r < 4; ++r) { auto sw = __builtin_amdgcn_permlane32_swap(fu(t[r]), fu(t[r]), false, false); t[r] = uf(sw[0]) + uf(sw[1]); }
    if (lane < 28) *(LAS f32x4*)(L + L_CS + type * 448 + lane * 16) = t;
  };
  auto wq_off = [&](int s, int r) {
    const int pc = r == 0 ? tid : (tid + 512 > 863 ? 863 : tid + 512), i = pc / 27, q = pc - i * 27;
    return unit_row(s, i) * 3456 + (q / 9) * 1080 + q * 8 + g0 * 72;
  };
  auto wq_put = [&](int img, v2u d0, v2u d1) { sts64(L, img + tid * 8, d0); sts64(L, img + (tid + 512 > 863 ? 863 : tid + 512) * 8, d1); };
  auto wq_emit = [&](int img, int type, int s) {
    const int w = w0, lane = lane0, tok = lane & 15, gq = lane >> 4, trq = (lane >> 2) & 3, trp = lane & 3;
    if (w < 7) {
      const int wco = (w & 1) * 40 + (w < 2 ? 72 : w < 4 ? 144 : 0) - (w > 4 ? 40 : 0) + (w == 6 ? 80 : 0);
      int a0 = img + (4 * gq + trq) * 216 + (w == 4 ? trp * 72 + 32 : wco + trp * 8), a1 = a0 + 16 * 216;
      if (w == 4 && trp == 3) a0 = a1 = L_ONEQ;
      const int j = w >= 5 ? 90 + 2 * s + w - 5 : 5 * s + w;
      const v4u f = cat(ldstr(L, a0), ldstr(L, a1));
      unsigned one2 = 0x3F803F80u; asm volatile("" : "+v"(one2));
      gst128(rs, WS_WQ + (type * 16 + g0) * WQ_BLOB + fr_slot(j) * 1024 + lane * 16, f);
      const f32x4 cs = mfma(f, v4u{one2, one2, one2, one2}, z);
      if (tok == 0) gst128(rs, WS_CSP + ((type * 16 + g0) * 18 + s) * 448 + w * 64 + gq * 16, __builtin_bit_cast(v4u, cs));
    }
  };
  auto wo_off = [&](int s) { const int po = tid > 287 ? 287 : tid, io = po / 9; return unit_row(s, io) * 1152 + g0 * 72 + (po - io * 9) * 8; };
  auto wo_emit = [&](int img, int type, int s, int t) {
    const int lane = lane0, tok = lane & 15, gq = lane >> 4, trq = (lane >> 2) & 3, trp = lane & 3, g = g0;
    const int a0 = img + (4 * gq + trq) * 72 + (t == 2 ? 32 : t * 40 + trp * 8);
    const int idx = s < 16 ? 2 * (s & 7) + 2 + (s >> 3) : s - 16;
    const int o = WS_WO + (type * 8 + (g >> 1)) * WO_BLOB + (17 - idx) * 4608 + (t < 2 ? (g & 1) * 2048 + t * 1024 + lane * 16 : 4096 + (gq * 8 + (g & 1) * 4 + tok) * 16);
    gst128(rs, (t == 2 && tok >= 4) ? OOB : o, cat(ldstr(L, a0), ldstr(L, a0 + 16 * 72)));
  };
  {
    const int lane = lane0, tok = lane & 15, gq = lane >> 4, w = w0, p = p0, g = g0, T = 8 * p + w;
    constexpr int RAW_A = 147456;
    v2u dA[2], dE[2], r0, r1, r2, ep, dP;
    aload64p(dA[0], wA, wq_off(p, 0)); aload64p(dA[1], wA, wq_off(p, 1));
    aload64(ep, ws, WS_EPOCH);
    aload64p(dE[0], wA, wq_off(16 + (p & 1), 0)); aload64p(dE[1], wA, wq_off(16 + (p & 1), 1));
    PIN();
    auto x_dma = [&](auto i_) {
      constexpr int i = decltype(i_)::value;
      const int ci = i * 64 + lane, r = (ci * 911) >> 16;
      const int src = T * 18432 + ci * 16 - r * 16 + (ci - r * 72 < r ? 1152 : 0);
      dma_in(x0, src, Lb + w * 18432 + i * 1024);
    };
    steps<0, 6>(x_dma);
    if (tid == 0) sts64(L, L_ONEQ, v2u{0x3F80u, 0u});
    VMWAIT(6); PIN();
    asm volatile("" : "+v"(dA[0]), "+v"(dA[1]), "+v"(dE[0]), "+v"(dE[1]), "+v"(ep));
    wq_put(RAW_A, dA[0], dA[1]);
    tag = ((u64)(unsigned)__builtin_amdgcn_readfirstlane(ep.x) | ((u64)(unsigned)__builtin_amdgcn_readfirstlane(ep.y) << 32)) + 1;
    BAR();
    wq_emit(RAW_A, 0, p);
    if (p < 2) { BAR(); wq_put(RAW_A, dE[0], dE[1]); BAR(); wq_emit(RAW_A, 0, 16 + p); }
    PIN();
    steps<6, 12>(x_dma);
    VMWAIT(6);
    RAWBAR();
    if (tid == 448) set_flag(ws, FL_RT1 + g, p, tag);
    TS(1);
    steps<12, 18>(x_dma);
    const int xr = (16 * (8 * p + 4 * (g & 1) + (w & 3)) + tok) * 1152 + 72 * ((g & ~1) + (w >> 2));
    aload64p(r0, x0, xr + 8 * gq); aload64p(r1, x0, xr + 40 + 8 * gq); aload64p(r2, x0, xr + 32);
    const int phi_i = tid < 162 ? tid : 161;
    aload64p(dP, wphi, g * 1296 + phi_i * 8);
    int xo[36];
    {
      const int xb = w * 18432 + tok * 1152;
      auto xoff = [&](int byte) { int c = (byte >> 4) + tok; c -= c >= 72 ? 72 : 0; return xb + c * 16 + (byte & 8); };
#pragma unroll
      for (int s = 0; s < 16; ++s) { xo[2 * s] = xoff(72 * s + 8 * gq); xo[2 * s + 1] = xoff(72 * s + 40 + 8 * gq); }
#pragma unroll
      for (int s = 0; s < 4; ++s) xo[32 + s] = xoff(72 * (8 * (s >> 1) + 2 * gq + (s & 1)) + 32);
#pragma unroll
      for (int i = 0; i < 36; ++i) asm volatile("" : "+v"(xo[i]));
    }
    VMWAIT(4);
#pragma unroll
    for (int s = 0; s < 18; ++s) xf[s] = cat(lds64(L, xo[2 * s]), lds64(L, xo[2 * s + 1]));
    TS(2);
    if (w == 0) poll(ws, poll_off(FL_RT1 + g, FL_RT1 + g, lane), true, tag);
    asm volatile("s_waitcnt vmcnt(0)" : "+v"(r0), "+v"(r1), "+v"(r2), "+v"(dP) :: "memory");
    BAR();
    TS(3);
    if (w == 7) colsums7(0);
#pragma unroll 1
    for (int k = 0; k < 8; ++k) dmaf(ws, WS_WQ + g * WQ_BLOB, Lb, fr_slot(k * 8 + w), lane * 16);
    { const int d0 = 2 * phi_i, d1 = d0 + 1; *(LAS unsigned*)(L + L_PH + (d0 / 9) * 4 + d0 * 4) = dP.x; *(LAS unsigned*)(L + L_PH + (d1 / 9) * 4 + d1 * 4) = dP.y; }
    if (tid >= 448 && tid < 448 + 46) { const int i = tid - 448; *(LAS unsigned*)(L + L_PH + (i < 36 ? i * 40 + 36 : 1440 + (i - 36) * 4)) = 0u; }
    res[0] = unpk4(r0); res[1] = unpk4(r1); res[2] = unpk4(r2);
    {
      float q4[4] = {0.f, 0.f, 0.f, 0.f};
#pragma unroll
      for (int s = 0; s < 18; ++s)
#pragma unroll
        for (int j = 0; j < 4; ++j) q4[j] = dot2(xf[s][j], q4[j]);
      ssq = (q4[0] + q4[1]) + (q4[2] + q4[3]);
    }
    PIN();
    VMWAIT(3);
    BAR();
    TS(4);
#pragma unroll
    for (int i = 0; i < RING; ++i) ring[i] = lds128(L, fr_slot(i) * 1024 + lane * 16);
    PIN();
  }

  auto layer = [&](auto ty_, auto first_, const int it) __attribute__((always_inline)) {
    constexpr int ty = decltype(ty_)::value;
    constexpr bool first = decltype(first_)::value;
    const int Lr = 2 * it + ty;
    int lane = lane0, w = w0, p = p0, g = g0;
    unsigned char* ws = ws0;
    asm volatile("" : "+v"(lane), "+s"(w), "+s"(p), "+s"(g), "+s"(ws));
    const int T = 8 * p + w;
    const int tok = lane & 15, gq = lane >> 4, trq = (lane >> 2) & 3, trp = lane & 3, l16 = lane * 16;
    const bool last = (ty == 1) && (it == 1);
    int hi16 = l16 + 65536;
    asm volatile("" : "+v"(hi16));
    auto wfrag = [&](int slot) { return slot < 64 ? lds128(L, l16 + slot * 1024) : lds128(L, hi16 + (slot - 64) * 1024); };
    const int curW = WS_WQ + (ty * 16 + g) * WQ_BLOB, nxtW = WS_WQ + ((1 - ty) * 16 + g) * WQ_BLOB, curO = WS_WO + (ty * 8 + (g >> 1)) * WO_BLOB;
    const int flm = FL_HOP + Lr * 48;
    v4u rv[4];
    const int xtb = WS_FX + (Lr - 1) * FRAG_BUF + T * FT, xv0 = xtb + 4096 + l16, xv1 = xv0 + 8192, xvr = xtb + 16384 + gq * 512 + tok * 16;
    auto x_op = [&](auto k_) {
      constexpr int k = decltype(k_)::value;
      if constexpr (k < 16) aload<(k % 8) * 1024 - 4096>(xf[k], ws, k < 8 ? xv0 : xv1);
      else if constexpr (k < 20) aload<((k - 16) & 1) * 256 + ((k - 16) >> 1) * 2048>(rv[k - 16], ws, xvr);
      else dmaf(ws, curW, Lb, min(90 + (k - 20) * 8 + w, 125), l16);
    };
    v2u dB[2], dE[2], dO[3];
    auto l0_op = [&](auto k_) {
      constexpr int k = decltype(k_)::value;
      if constexpr (k < 8) dmaf(ws, curW, Lb, fr_slot(min((k + 8) * 8 + w, 125)), l16);
      else if constexpr (k < 10) aload64p(dB[k - 8], wB, wq_off(p, k - 8));
      else if constexpr (k < 12) aload64p(dE[k - 10], wB, wq_off(16 + (p & 1), k - 10));
      else if constexpr (k < 14) aload64p(dO[k - 12], k == 12 ? woA : woB, wo_off(p));
      else if constexpr (k == 14) aload64p(dO[2], p < 6 ? woA : woB, wo_off(16 + (p & 1)));
    };
    if (!first) steps<0, xn(-1)>(x_op);
    PIN();
    f32x4 acc[7];
#pragma unroll
    for (int t = 0; t < 7; ++t) acc[t] = z;
    constexpr int PI = 112;
    v2u pollv;
    constexpr int RAW_B = 8192, RAW_OA = RAW_B + 6912, RAW_OB = RAW_OA + 2304, RAW_E1 = RAW_OB + 2304;
    auto qkv_step = [&](auto i_) {
      constexpr int i = decltype(i_)::value, s = fr_s(i), t = fr_t(i);
      if (i < 80 && t == 0) {
        PIN();
        if (!first) { steps<xn(s - 1), xn(s)>(x_op); PIN(); GOT(xn(s) - s - 1, xf[s]); }
        else {
          l0_op(IC(s));
          if (s == 5) { VMWAIT(6); RAWBAR(); }
          if (s == 10) { VMWAIT(3); RAWBAR(); }
          PIN();
        }
      }
      if (i == 80 && !first) {
        PIN();
        asm volatile("s_waitcnt vmcnt(5)" : "+v"(rv[0]), "+v"(rv[1]), "+v"(rv[2]), "+v"(rv[3]) :: "memory");
        xf[16] = v4u{rv[0].x, rv[0].y, rv[1].x, rv[1].y}; xf[17] = v4u{rv[2].x, rv[2].y, rv[3].x, rv[3].y};
        ssq = (uf(rv[0].z) + uf(rv[1].z)) + (uf(rv[2].z) + uf(rv[3].z));
      }
      if (i == 85 && first) {
        PIN();
        asm volatile("s_waitcnt vmcnt(0)" : "+v"(dB[0]), "+v"(dB[1]), "+v"(dE[0]), "+v"(dE[1]), "+v"(dO[0]), "+v"(dO[1]), "+v"(dO[2]) :: "memory");
        const int po = (tid > 287 ? 287 : tid) * 8;
        wq_put(RAW_B, dB[0], dB[1]); sts64(L, RAW_OA + po, dO[0]); sts64(L, RAW_OB + po, dO[1]);
        if (p < 4) wq_put(RAW_E1, dE[0], dE[1]); else sts64(L, RAW_E1 + po, dO[2]);
        PIN();
      }
      if (i == PI && ty == 1) { PIN(); aload64(pollv, ws, poll_off(flm + g, flm + (first ? g ^ 1 : g), lane)); PIN(); }
      acc[t] = mfma(ring[i % RING], xf[s], acc[t]);
      if (i + RING < 126 && (i + RING < 90 || i >= 90)) ring[i % RING] = wfrag(fr_slot(i + RING));
      PINM();
    };
    const int csb = L_CS + ty * 448 + gq * 16;
    auto wph = [&](int h, int ft) {
      const int rl = L_PH + (20 * h + 4 * gq + trq) * 40, ru = L_PH + ((trq >> 1) == h ? (18 * h + (h ? 0 : 16) + (trq & 1)) * 40 : 1440);
      return cat(ldstr(L, rl + (ft ? 32 : trp * 8)), ldstr(L, ru + (ft ? 32 : trp * 8)));
    };
    const int vsw = gq * 128 + ((tok * 8) ^ ((gq >> 1) * 64));
    const int trl = trp * 128 + (((4 * gq + trq) * 8) ^ ((trp >> 1) * 64));
    v4u wp[2][2]; f32x4 cs[5];
    steps<0, 78>(qkv_step);
#pragma unroll
    for (int t = 0; t < 5; ++t) cs[t] = *(LAS f32x4*)(L + csb + t * 64);
    if (ty == 0) {
#pragma unroll
      for (int h = 0; h < 2; ++h) { wp[h][0] = wph(h, 0); wp[h][1] = wph(h, 1); }
    }
    PIN();
    steps<78, 90>(qkv_step);
    const float mean = hi_half(odd_rows(acc[4].x)) * (1.0f / 576.0f);
    const float var = red_add(ssq) * (1.0f / 576.0f) - mean * mean;
    const float rstd = __builtin_amdgcn_rsqf(fmaxf(var, 0.f) + 1e-5f);
    auto fix = [&](int t, float sc) { return (acc[t] - mean * (t < 5 ? cs[t < 5 ? t : 0] : *(LAS f32x4*)(L + csb + t * 64))) * sc; };
    v2u kq[2], vq[2];
    kq[0] = pk4(fix(0, rstd)); kq[1] = pk4(fix(1, rstd));
    const f32x4 remf = fix(4, rstd);
    const v2u remq = pk4(remf);
    const int vpos = gq * 256 + ((tok * 16) ^ ((gq >> 1) * 128));
    v4u kboth, vboth;
    const int kh = w >= 3 ? 1 : 0, kft = w - 3 * kh;
    if (ty == 0) {
      const v2u ku = gq == 1 ? remq : v2u{0u, 0u};
      f32x4 pv[2][2];
#pragma unroll
      for (int h = 0; h < 2; ++h) { const v4u bk = cat(kq[h], ku); pv[h][0] = mfma(wp[h][0], bk, z); pv[h][1] = mfma(wp[h][1], bk, z); }
      const int sb = L_STG + w * STG_REC;
      vq[0] = pk4(fix(2, rstd)); vq[1] = pk4(fix(3, rstd));
      sts64(L, sb + vsw, vq[0]); sts64(L, sb + 512 + vsw, vq[1]);
      if (gq == 2) sts64(L, sb + 3072 + tok * 8, remq);
      if (lane >= 62) sts64(L, sb + 3456 + (lane & 1) * 8, v2u{(lane & 1) ? 0u : 0x3F80u, 0u});
      PIN();
      v2u pl[2], ml[2], hq[2];
      phi_pair(pv, gq, pl, ml, hq);
#pragma unroll
      for (int h = 0; h < 2; ++h) {
        sts64(L, sb + 1024 + h * 1024 + vsw, pl[h]); sts64(L, sb + 1536 + h * 1024 + vsw, ml[h]);
        if (gq == 0) sts64(L, sb + 3200 + h * 128 + tok * 8, hq[h]);
      }
      TS(8 + Lr * 12 + 0);
      DRAIN();
      BAR();
      if (w < 6) {
        const int aa = L_STG + (kft < 2 ? 1024 + kh * 1024 + kft * 512 + trl : (trp == 0 ? 3200 + kh * 128 + (4 * gq + trq) * 8 : 3464));
        const int bl = L_STG + kh * 512 + trl, bh = L_STG + (trp == 0 ? 3072 + (4 * gq + trq) * 8 : 3456);
        v4u fa[4], fl[4], fh[4];
#pragma unroll
        for (int ks = 0; ks < 4; ++ks) {
          const int o0 = 2 * ks * STG_REC, o1 = o0 + STG_REC;
          fa[ks] = cat(ldstr(L, aa + o0), ldstr(L, aa + o1)); fl[ks] = cat(ldstr(L, bl + o0), ldstr(L, bl + o1)); fh[ks] = cat(ldstr(L, bh + o0), ldstr(L, bh + o1));
        }
        PIN();
        f32x4 dl = z, dh = z;
#pragma unroll
        for (int ks = 0; ks < 4; ++ks) { dl = mfma(fa[ks], fl[ks], dl); dh = mfma(fa[ks], fh[ks], dh); }
        const int rb = WS_KVP + ((it * 16 + g) * 16 + p) * KVP_REC + kh * 2880;
        const int slot = tok == 4 ? 2 : (tok >> 1) == kh ? (tok & 1) : -1;
        const bool rowok = kft < 2 || gq == 0;
        if (rowok) gst128(rs, rb + kft * 1024 + l16, __builtin_bit_cast(v4u, dl));
        if (rowok && slot >= 0) gst128(rs, rb + 2304 + kft * 256 + (gq * 4 + slot) * 16, __builtin_bit_cast(v4u, dh));
      } else {
#pragma unroll
        for (int j = 0; j < 4; ++j) { unsigned zz = 0u; asm volatile("" : "+v"(zz)); sts128(L, (w - 6) * 4096 + j * 1024 + l16, v4u{zz, zz, zz, zz}); }
      }
      if (first) {
        wq_emit(RAW_B, 1, p);
        if (p >= 2 && p < 4) wq_emit(RAW_E1, 1, 16 + (p & 1));
        if (w < 3) wo_emit(RAW_OA, 0, p, w); else if (w < 6) wo_emit(RAW_OB, 1, p, w - 3);
        if (p >= 4 && p < 8 && w < 3) wo_emit(RAW_E1, p < 6 ? 0 : 1, 16 + (p & 1), w);
      }
    } else {
      const int rb = WS_KVX + it * KVX_BUF + (T * 16 + g) * KVX_REC;
      gst128(rs, rb + l16, kboth = (gq & 1) ? cat(kq[1], kq[0]) : cat(kq[0], kq[1]));
      if (gq == 1 || gq == 2) gst64(rs, rb + 2048 + (gq - 1) * 128 + tok * 8, remq);
      vq[0] = pk4(fix(2, rstd)); vq[1] = pk4(fix(3, rstd));
      TS(8 + Lr * 12 + 0);
      gst128(rs, rb + 1024 + vpos, vboth = (gq & 1) ? cat(vq[1], vq[0]) : cat(vq[0], vq[1]));
      VMWAIT(3);
      RAWBAR();
      const int ib = (16 + w) * IMG_REC;
      sts128(L, ib + l16, kboth); sts128(L, ib + 1024 + vpos, vboth);
      if (gq == 1 || gq == 2) sts64(L, ib + 2048 + (gq - 1) * 128 + tok * 8, remq);
      if (lane < 3) { const int i = w + 8 * lane; sts128(L, i * IMG_REC + 2304, v4u{0x3F80u, 0u, (8 * (p - 2) + i >= 0) ? 0u : 0xF149u, 0u}); }
    }
    PIN();
#pragma unroll
    for (int j = 90; j < 90 + RING; ++j) ring[j % RING] = wfrag(fr_slot(j));
    PIN();
    DRAIN();
    RAWBAR();
    if (tid == 448) set_flag(ws, flm + g, p, tag);
    TS(8 + Lr * 12 + 1);
    PIN();
    steps<90, 126>(qkv_step);
    auto poll_done = [&](bool care) {
      DRAIN(); PIN();
      asm volatile("" : "+v"(pollv));
      if (!__all(!care || ((u64)pollv.y << 32 | pollv.x) == tag)) poll(ws, poll_off(flm + g, flm + (first ? g ^ 1 : g), lane), care, tag);
    };
    TS(8 + Lr * 12 + 2);
    v4u dp[16];
    const int pi = w * 64 + lane;
    if (ty == 1) {
      poll_done(tok == p - 1 || tok == p - 2);
#pragma unroll
      for (int j = 0; j < 2; ++j) {
        const int Tg = 8 * (p - 2) + w + 8 * j, ib = (w + 8 * j) * IMG_REC;
        if (Tg >= 0) {
          const int rb = WS_KVX + it * KVX_BUF + (Tg * 16 + g) * KVX_REC;
          dmaf(ws, rb, Lb + ib, 0, l16); dmaf(ws, rb, Lb + ib, 1, l16);
          if (lane < 16) dmaf(ws, rb, Lb + ib, 2, l16);
        } else {
          unsigned zz = 0u; asm volatile("" : "+v"(zz));
          sts128(L, ib + l16, v4u{zz, zz, zz, zz}); sts128(L, ib + 1024 + l16, v4u{zz, zz, zz, zz});
          if (lane < 16) sts128(L, ib + 2048 + l16, v4u{zz, zz, zz, zz});
        }
      }
    }
    PIN();
    if (ty == 1) TS(8 + Lr * 12 + 3);
    constexpr float QS = 0.23570226039551584f * LOG2E;
    const v2u q0 = pk4(fix(5, ty == 1 ? rstd * QS : rstd)), q1 = pk4(fix(6, ty == 1 ? rstd * QS : rstd));
    v2u olo[2]; unsigned ohi[2];
    if (ty == 0) {
      v4u qf[2][2];
      {
        const v2u qu = gq == 0 ? remq : v2u{0u, 0u};
        f32x4 pv[2][2];
#pragma unroll
        for (int h = 0; h < 2; ++h) { const v4u bq = cat(h ? q1 : q0, qu); pv[h][0] = mfma(wp[h][0], bq, z); pv[h][1] = mfma(wp[h][1], bq, z); }
        PIN();
        if (w == 0) aload64(pollv, ws, poll_off(flm + g, flm + (first ? g ^ 1 : g), lane));
        PIN();
        v2u pl[2], ml[2], hq[2];
        phi_pair(pv, gq, pl, ml, hq);
#pragma unroll
        for (int h = 0; h < 2; ++h) { qf[h][0] = cat(pl[h], ml[h]); qf[h][1] = cat(hq[h], v2u{0u, 0u}); }
      }
      PIN();
      if (w == 0) poll_done(true);
      RAWBAR();
      TS(8 + Lr * 12 + 3);
      if (w >= 6) {
        __builtin_amdgcn_s_sleep(18);
#pragma unroll 1
        for (int f = O_LATE - 1 + w - 6; f < 81; f += 2) dmaf(ws, curO, Lb + L_O, f, l16);
      } else {
        const int po = pi < 360 ? WS_KVP + (it * 16 + g) * 16 * KVP_REC + pi * 16 : OOB;
#pragma unroll
        for (int q = 0; q < 16; ++q) dp[q] = gld128(rs, po, q * KVP_REC);
        PIN();
        f32x4 s0 = __builtin_bit_cast(f32x4, dp[0]), s1 = __builtin_bit_cast(f32x4, dp[1]);
#pragma unroll
        for (int q = 2; q < 16; q += 2) { s0 += __builtin_bit_cast(f32x4, dp[q]); s1 += __builtin_bit_cast(f32x4, dp[q + 1]); }
        const v2u sv = pk4(s0 + s1);
        const int h = pi >= 180 ? 1 : 0, j = pi - 180 * h;
        const int e = j - 144, sl = e & 3, half = j < 144 ? (j >> 6) & 1 : (e >> 4) & 1;
        const int blk = j < 128 ? 0 : j < 144 ? 1024 : e < 32 ? 2048 : 3072;
        const int ln = j < 144 ? (j & 63) : ((e >> 2) & 3) * 16 + (sl < 2 ? 2 * h + sl : 4);
        const int dst = h * 4096 + blk + ln * 16 + half * 8;
        if (pi < 360 && (j < 144 || sl < 3)) sts64(L, dst, sv);
        if (pi < 360 && j >= 144 && sl == 2) sts64(L, dst + 128, sv);
      }
      BAR();
      TS(8 + Lr * 12 + 4);
      v4u kf[2][4];
#pragma unroll
      for (int h = 0; h < 2; ++h)
#pragma unroll
        for (int j = 0; j < 4; ++j) kf[h][j] = lds128(L, h * 4096 + j * 1024 + l16);
      PIN();
#pragma unroll
      for (int h = 0; h < 2; ++h) {
        const f32x4 dl2 = mfma(kf[h][1], qf[h][1], mfma(kf[h][0], qf[h][0], z));
        const f32x4 dh2 = mfma(kf[h][3], qf[h][1], mfma(kf[h][2], qf[h][0], z));
        const float r = __builtin_amdgcn_rcpf(odd_rows(dh2.x) + 1e-6f);
        olo[h] = pk4(dl2 * r); ohi[h] = h == 0 ? pk2(dh2.x * r, dh2.y * r) : pk2(dh2.z * r, dh2.w * r);
      }
    } else {
      v4u qf[2];
      {
        const v2u qr = pk4(remf * QS);
        const unsigned bias1 = gq == 1 ? 0x3F80u : 0u;
        qf[0] = cat(q0, v2u{gq == 0 ? qr.x : bias1, 0u});
        qf[1] = cat(q1, v2u{bias1, gq == 0 ? qr.y : 0u});
      }
      const int wb = w * IMG_REC;
      const int kuo = wb + (gq == 1 ? 2312 : 2048 + tok * 8);
      auto kfrag = [&](int h, int i) { return cat(lds64(L, wb + ((h ^ (gq & 1)) * 8) + l16 + i * IMG_REC), lds64(L, kuo + i * IMG_REC)); };
      float nm[2];
#pragma unroll
      for (int h = 0; h < 2; ++h) {
        const f32x4 sd = mfma(cat(kq[h], gq == 1 ? v2u{0u, 0u} : lds64(L, kuo + 16 * IMG_REC)), qf[h], z);
        float m = (4 * gq <= tok) ? sd.x : -3e38f;
#pragma unroll
        for (int r = 1; r < 4; ++r) m = fmaxf(m, (4 * gq + r <= tok) ? sd[r] : -3e38f);
        nm[h] = -red_max(m);
      }
      const int vl = wb + 1024 + trp * 256 + (((4 * gq + trq) * 16) ^ ((trp >> 1) * 128));
      const int vh = wb + (trp == 0 ? 2176 + (4 * gq + trq) * 8 : 2304);
      const unsigned m0 = (4 * gq + 0 <= tok ? 0xffffu : 0u) | (4 * gq + 1 <= tok ? 0xffff0000u : 0u);
      const unsigned m1 = (4 * gq + 2 <= tok ? 0xffffu : 0u) | (4 * gq + 3 <= tok ? 0xffff0000u : 0u);
      PIN();
      DRAIN();
      BAR();
      TS(8 + Lr * 12 + 4);
#pragma unroll 1
      for (int pass = 0; pass < 2; ++pass) {
        v4u kf[34]; f32x4 sc[2][17]; v4u va, vb, van, vbn; f32x4 dl, dh;
        bool ok = true;
        constexpr int ord[17] = {16, 0, 1, 2, 3, 4, 5, 6, 7, 8, 9, 10, 11, 12, 13, 14, 15};
#pragma unroll
        for (int j = 0; j < 8; ++j) kf[j] = kfrag(0, ord[j]);
        auto vread = [&](int h, int ks) {
          const int i0 = ks == 0 ? 0 : 2 * ks - 1, i1 = ks == 0 ? 16 : ks < 8 ? 2 * ks : 15, vo = vl + ((h ^ (trp & 1)) * 8);
          van = cat(ldstr(L, vo + i0 * IMG_REC), ldstr(L, vo + i1 * IMG_REC));
          vbn = cat(ldstr(L, vh + i0 * IMG_REC), ldstr(L, vh + i1 * IMG_REC));
        };
        auto qk = [&](int j) { const int h = j / 17; sc[h][ord[j % 17]] = mfma(kf[j], qf[h], f32x4{nm[h], nm[h], nm[h], nm[h]}); };
        auto pexp = [&](f32x4 x) {
          f32x4 e;
#pragma unroll
          for (int r = 0; r < 4; ++r) e[r] = __builtin_amdgcn_exp2f(x[r]);
          return pk4(e);
        };
        vread(0, 0);
        PIN();
#pragma unroll
        for (int j = 0; j < 4; ++j) qk(j);
        PIN();
        auto att_step = [&](auto n_) {
          constexpr int n = decltype(n_)::value, h = n / 9, ks = n % 9;
          va = van; vb = vbn;
          if (n < 17) vread((n + 1) / 9, (n + 1) % 9);
          if (2 * n + 8 < 34) kf[2 * n + 8] = kfrag((2 * n + 8) / 17, ord[(2 * n + 8) % 17]);
          if (2 * n + 9 < 34) kf[2 * n + 9] = kfrag((2 * n + 9) / 17, ord[(2 * n + 9) % 17]);
          if (2 * n + 4 < 34) qk(2 * n + 4);
          if (2 * n + 5 < 34) qk(2 * n + 5);
          v4u bp;
          if (ks == 0) {
            f32x4 mg;
#pragma unroll
            for (int r = 0; r < 4; ++r) mg[r] = (4 * gq + r <= tok) ? sc[h][16][r] : sc[h][0][r];
            const v2u p0 = pexp(mg);
            bp = v4u{p0.x & ~m0, p0.y & ~m1, p0.x & m0, p0.y & m1};
          } else bp = cat(pexp(sc[h][2 * ks - 1]), ks < 8 ? pexp(sc[h][ks < 8 ? 2 * ks : 0]) : v2u{0u, 0u});
          dl = mfma(va, bp, ks == 0 ? z : dl); dh = mfma(vb, bp, ks == 0 ? z : dh);
          int wo_ = w; asm volatile("" : "+s"(wo_));
          if (n < 11) dmaf(ws, curO, Lb + L_O, min(n * 8 + wo_, 80), l16);
          else if (n < 14 && !last) dmaf(ws, nxtW, Lb, min(55 + (n - 11) * 8 + wo_, 75), l16);
          if (ks == 8) {
            const float l = odd_rows(dh.x);
            ok = ok && l < BIG;
            const float r = __builtin_amdgcn_rcpf(l);
            olo[h] = pk4(dl * r); ohi[h] = h == 0 ? pk2(dh.x * r, dh.y * r) : pk2(dh.z * r, dh.w * r);
          }
          PIN();
        };
        steps<0, 18>(att_step);
        if (__builtin_expect(__all(ok), 1)) break;
#pragma unroll
        for (int h = 0; h < 2; ++h) {
          const f32x4 s0 = mfma(kfrag(h, 0), qf[h], z), s16 = mfma(kfrag(h, 16), qf[h], z);
          float m = -3e38f;
#pragma unroll
          for (int r = 0; r < 4; ++r) m = fmaxf(m, (4 * gq + r <= tok) ? s16[r] : s0[r]);
#pragma unroll 1
          for (int i = 1; i < 16; ++i) { const f32x4 s = mfma(kfrag(h, i), qf[h], z); m = fmaxf(fmaxf(fmaxf(m, s.x), fmaxf(s.y, s.z)), s.w); }
          nm[h] = -red_max(m);
        }
      }
    }
    TS(8 + Lr * 12 + 5);
    gst128(rs, WS_FO + Lr * FRAG_BUF + T * FT + g * 1024 + l16, cat(olo[0], olo[1]));
    gst128(rs, gq == 0 ? WS_FO + Lr * FRAG_BUF + T * FT + 16384 + g * 256 + tok * 16 : OOB, v4u{ohi[0], ohi[1], 0u, 0u});
    const int kw = w >> 2, To = 8 * p + 4 * (g & 1) + (w & 3), go = (g & ~1) + kw;
    int omine = L_O + (1 - kw) * 4608 + kw * 2048 + l16, oother = L_O + (1 - kw) * 4608 + (1 - kw) * 2048 + l16, orem = L_O + (1 - kw) * 4608 + 4096 + (gq * 8 + (tok & 7)) * 16;
    asm volatile("" : "+v"(omine), "+v"(oother), "+v"(orem));
    auto ofrag = [&](int i) { const int u = i / 5, t = i % 5; return lds128(L, (8 - u) * 9216 + (t < 2 ? omine + t * 1024 : t < 4 ? oother + (t - 2) * 1024 : orem)); };
    DRAIN();
    RAWBAR();
    if (tid == 448) set_flag(ws, flm + 16 + p, g, tag);
    TS(8 + Lr * 12 + 6);
    if (ty == 0) dma_range<7>(ws, curO, Lb + L_O, 0, O_LATE - 1, w, l16);
    if (first && w == 7) colsums7(1);
#pragma unroll
    for (int i = 0; i < 15; ++i) ring[i] = ofrag(i);
    PIN();
    if (ty == 0) DRAIN();
    if (w == 0) poll(ws, poll_off(flm + 16 + p, flm + 16 + p, lane), true, tag);
    RAWBAR();
    TS(8 + Lr * 12 + 7);
    const int ob = WS_FO + Lr * FRAG_BUF + To * FT, ov0 = ob + kw * 8192 + 4096 + l16, ovr = ob + 16384 + kw * 2048 + gq * 512 + tok * 16;
    auto o_op = [&](auto k_) {
      constexpr int k = decltype(k_)::value;
      if constexpr (k < 2) aload<k * 256>(rv[k], ws, ovr);
      else if constexpr (k < 10) aload<(k - 2) * 1024 - 4096>(xf[k - 1], ws, ov0);
      else if constexpr (k < 14) dmaf(ws, nxtW, Lb, min((k - 10) * 8 + w, 30), l16);
      else dmaf(ws, nxtW, Lb, min(55 + (k - 14) * 8 + w, 75), l16);
    };
    steps<0, on(ty, -1)>(o_op);
    PIN();
    f32x4 oa[5] = {res[0], res[1], z, z, z};
    auto wo_step = [&](auto i_) {
      constexpr int i = decltype(i_)::value, u = i / 5, t = i % 5;
      if (t == 0) {
        PIN(); steps<on(ty, u - 1), on(ty, u)>(o_op); PIN();
        if (u == 0) {
          asm volatile("s_waitcnt vmcnt(%2)" : "+v"(rv[0]), "+v"(rv[1]) : "n"(on(ty, 0) - 2) : "memory");
          xf[0] = v4u{rv[0].x, rv[0].y, rv[1].x, rv[1].y};
        } else GOT(on(ty, u) - u - 2, xf[u]);
      }
      oa[t] = mfma(ring[i % 15], xf[u], oa[t]);
      if (i + 15 < 45) ring[i % 15] = ofrag(i + 15);
      PINM();
    };
    steps<0, 45>(wo_step);
    {
      const int xm = L_XCH + w * 3072 + l16, xo = L_XCH + (w ^ 4) * 3072 + l16;
      sts128(L, xm, __builtin_bit_cast(v4u, oa[2])); sts128(L, xm + 1024, __builtin_bit_cast(v4u, oa[3])); sts128(L, xm + 2048, __builtin_bit_cast(v4u, oa[4]));
      BAR();
      res[0] = oa[0] + __builtin_bit_cast(f32x4, lds128(L, xo)); res[1] = oa[1] + __builtin_bit_cast(f32x4, lds128(L, xo + 1024));
      f32x4 rm = oa[4] + __builtin_bit_cast(f32x4, lds128(L, xo + 2048));
#pragma unroll
      for (int r = 0; r < 4; ++r) { auto sw = __builtin_amdgcn_permlane16_swap(fu(rm[r]), fu(rm[r]), false, false); rm[r] = uf(kw ? sw[1] : sw[0]); }
      res[2] += rm;
    }
    TS(8 + Lr * 12 + 8);
    const v2u xa = pk4(res[0]), xb = pk4(res[1]), xc = pk4(res[2]);
    if (!last) {
      const float sq = red_add(dot2(xa.x, dot2(xa.y, 0.f)) + dot2(xb.x, dot2(xb.y, 0.f))) + dot2(xc.x, dot2(xc.y, 0.f));
      gst128(rs, WS_FX + Lr * FRAG_BUF + To * FT + go * 1024 + l16, cat(xa, xb));
      gst128(rs, gq == 0 ? WS_FX + Lr * FRAG_BUF + To * FT + 16384 + go * 256 + tok * 16 : OOB, v4u{xc.x, xc.y, fu(sq), 0u});
      DRAIN();
      RAWBAR();
      if (tid == 448) set_flag(ws, flm + 32 + p, g, tag);
      TS(8 + Lr * 12 + 9);
      dma_range<7>(ws, nxtW, Lb, 31, 55, w, l16);
      dma_range<7>(ws, nxtW, Lb, 76, 90, w, l16);
#pragma unroll
      for (int i = 0; i < RING; ++i) ring[i] = wfrag(fr_slot(i));
      PIN();
      DRAIN();
      if (w == 0) poll(ws, poll_off(flm + 32 + p, flm + 32 + p, lane), true, tag);
      RAWBAR();
      TS(8 + Lr * 12 + 10);
    } else {
      GAS unsigned char* orow = (GAS unsigned char*)out + (16 * To + tok) * 1152 + 72 * go;
      *(GAS v2u*)(orow + 8 * gq) = xa;
      *(GAS v2u*)(orow + 40 + 8 * gq) = xb;
      if (gq == 0) *(GAS v2u*)(orow + 32) = xc;
      if (b == 0 && tid == 0) __hip_atomic_store((GAS u64*)(ws + WS_EPOCH), tag, RLX_AGENT);
      DRAIN();
      TS(8 + Lr * 12 + 9);
    }
  };
  layer(IC(0), IC(1), 0);
#pragma unroll 1
  for (int it = 0; it < 2; ++it) {
    layer(IC(1), IC(0), it);
    if (it == 0) layer(IC(0), IC(0), 1);
  }
}

extern "C" void kernel_launch(void* const* d_in, const int* in_sizes, int n_in, void* d_out, int out_size, void* d_ws, size_t ws_size, hipStream_t stream) {
  (void)hipFuncSetAttribute((const void*)fused_kernel, hipFuncAttributeMaxDynamicSharedMemorySize, L_TOTAL);
  fused_kernel<<<256, 512, L_TOTAL, stream>>>((const u16*)d_in[0], (const u16*)d_in[1], (const u16*)d_in[2], (const u16*)d_in[3],
                                              (const u16*)d_in[5], (const u16*)d_in[6], (u16*)d_out, (unsigned char*)d_ws);
}
```

```cpp
#include <hip/hip_runtime.h>
#include <type_traits>
#include <utility>
typedef unsigned short u16;
typedef unsigned long long u64;
typedef unsigned v4u __attribute__((ext_vector_type(4)));
typedef unsigned v2u __attribute__((ext_vector_type(2)));
typedef float f32x4 __attribute__((ext_vector_type(4)));
typedef float f32x2 __attribute__((ext_vector_type(2)));
typedef short bf16x8 __attribute__((ext_vector_type(8)));
typedef short s16x4 __attribute__((ext_vector_type(4)));
typedef __bf16 bf16x2 __attribute__((ext_vector_type(2)));
#define GAS __attribute__((address_space(1)))
#define LAS __attribute__((address_space(3)))
#define RLX_AGENT __ATOMIC_RELAXED, __HIP_MEMORY_SCOPE_AGENT
#define DEV __device__ __forceinline__
#define IC(n) std::integral_constant<int, (n)>{}

constexpr int WS_EPOCH = 0;
constexpr int WS_FLAGS = 4096;
constexpr int FL_RT1 = 0, FL_CP = 16, FL_HOP = 32;
constexpr int WQ_BLOB = 129024, WO_BLOB = 82944;
constexpr int WS_WQ = 1 << 20;
constexpr int WS_WO = WS_WQ + 2 * 16 * WQ_BLOB;
constexpr int FT = 20480;
constexpr int FRAG_BUF = 128 * FT;
constexpr int WS_FX = 8 << 20;
constexpr int WS_FO = WS_FX + 3 * FRAG_BUF;
constexpr int KVX_REC = 2304;
constexpr int KVX_BUF = 128 * 16 * KVX_REC;
constexpr int WS_KVX = WS_FO + 4 * FRAG_BUF;
constexpr int KVP_REC = 5760;
constexpr int WS_KVP = WS_KVX + 2 * KVX_BUF;
constexpr int WS_CSP = WS_KVP + 2 * 256 * KVP_REC;
constexpr int WS_DBG = WS_CSP + 2 * 16 * 18 * 448;
constexpr int WS_TOTAL = WS_DBG + 256 * 64 * 8 * 10;
static_assert(WS_WO + 2 * 8 * WO_BLOB <= WS_FX, "ws map");
constexpr int L_O = 77824, L_TOTAL = 163840;
constexpr int O_LATE = 50;
constexpr int L_STG = 8192;
constexpr int L_XCH = 0;
constexpr int IMG_REC = 2320, STG_REC = 3472;
constexpr int L_CS = L_O + WO_BLOB;
constexpr int L_PH = L_CS + 896;
constexpr int L_ONEQ = L_TOTAL - 16;
static_assert(L_PH + 1480 <= L_ONEQ && L_PH % 8 == 0 && L_O + O_LATE * 1024 == WQ_BLOB && 24 * IMG_REC <= 55 * 1024, "lds map");
constexpr int OOB = 0x7ffffff0;
constexpr unsigned SPIN_CAP = 1u << 14;
constexpr float LOG2E = 1.4426950408889634f;
constexpr float BIG = 18446744073709551616.0f;
constexpr int RING = 14;
constexpr unsigned SENT = 0xFFFFFFFFu;
#define HOP_O 30
#define HOP_X 30
#define RETRY_SLEEP 8

DEV f32x4 mfma(v4u a, v4u b, f32x4 c) {
  return __builtin_amdgcn_mfma_f32_16x16x32_bf16(__builtin_bit_cast(bf16x8, a), __builtin_bit_cast(bf16x8, b), c, 0, 0, 0);
}
DEV unsigned pk2(float lo, float hi) { f32x2 v = {lo, hi}; return __builtin_bit_cast(unsigned, __builtin_convertvector(v, bf16x2)); }
DEV v2u pk4(f32x4 v) { return v2u{pk2(v.x, v.y), pk2(v.z, v.w)}; }
DEV unsigned fu(float f) { return __builtin_bit_cast(unsigned, f); }
DEV float uf(unsigned u) { return __builtin_bit_cast(float, u); }
DEV f32x4 unpk4(v2u u) { return f32x4{uf(u.x << 16), uf(u.x & 0xffff0000u), uf(u.y << 16), uf(u.y & 0xffff0000u)}; }
DEV float dot2(unsigned a, float c) { return __builtin_amdgcn_fdot2_f32_bf16(__builtin_bit_cast(bf16x2, a), __builtin_bit_cast(bf16x2, a), c, false); }
DEV v4u cat(v2u a, v2u b) { return v4u{a.x, a.y, b.x, b.y}; }
DEV float red_add(float x) { auto a = __builtin_amdgcn_permlane16_swap(fu(x), fu(x), false, false); x = uf(a[0]) + uf(a[1]); auto b = __builtin_amdgcn_permlane32_swap(fu(x), fu(x), false, false); return uf(b[0]) + uf(b[1]); }
DEV float red_max(float x) { auto a = __builtin_amdgcn_permlane16_swap(fu(x), fu(x), false, false); x = fmaxf(uf(a[0]), uf(a[1])); auto b = __builtin_amdgcn_permlane32_swap(fu(x), fu(x), false, false); return fmaxf(uf(b[0]), uf(b[1])); }
DEV float red_min(float x) { auto a = __builtin_amdgcn_permlane16_swap(fu(x), fu(x), false, false); x = fminf(uf(a[0]), uf(a[1])); auto b = __builtin_amdgcn_permlane32_swap(fu(x), fu(x), false, false); return fminf(uf(b[0]), uf(b[1])); }
DEV float odd_rows(float x) { auto a = __builtin_amdgcn_permlane16_swap(fu(x), fu(x), false, false); return uf(a[1]); }
DEV float hi_half(float x) { auto a = __builtin_amdgcn_permlane32_swap(fu(x), fu(x), false, false); return uf(a[1]); }

typedef LAS unsigned char* lptr;
DEV v4u lds128(lptr L, int off) { return *(LAS v4u*)(L + off); }
DEV v2u lds64(lptr L, int off) { return *(LAS v2u*)(L + off); }
DEV v2u ldstr(lptr L, int off) { return __builtin_bit_cast(v2u, __builtin_amdgcn_ds_read_tr16_b64_v4i16((LAS s16x4*)(L + off))); }
DEV void sts128(lptr L, int off, v4u v) { *(LAS v4u*)(L + off) = v; }
DEV void sts64(lptr L, int off, v2u v) { *(LAS v2u*)(L + off) = v; }
#define BAR() do { asm volatile("s_waitcnt lgkmcnt(0)" ::: "memory"); __builtin_amdgcn_s_barrier(); asm volatile("" ::: "memory"); } while (0)
#define RAWBAR() do { asm volatile("" ::: "memory"); __builtin_amdgcn_s_barrier(); asm volatile("" ::: "memory"); } while (0)
#define VMWAIT(n) asm volatile("s_waitcnt vmcnt(%0)" :: "n"(n) : "memory")
#define DRAIN() VMWAIT(0)
#define PIN() __builtin_amdgcn_sched_barrier(0)
#define PINM() __builtin_amdgcn_sched_barrier(0x406)
typedef __amdgpu_buffer_rsrc_t rsrc_t;
DEV v4u gld128(rsrc_t r, int off, int soff = 0) { return __builtin_amdgcn_raw_buffer_load_b128(r, off, soff, 16); }
DEV void gst128(rsrc_t r, int off, v4u v) { __builtin_amdgcn_raw_buffer_store_b128(v, r, off, 0, 16); }
DEV void gst64(rsrc_t r, int off, v2u v) { __builtin_amdgcn_raw_buffer_store_b64(v, r, off, 0, 16); }
template <int OFF> DEV void aload(v4u& d, const unsigned char* sbase, int voff) { asm volatile("global_load_dwordx4 %0, %1, %2 offset:%3 sc1" : "=v"(d) : "v"(voff), "s"(sbase), "n"(OFF) : "memory"); }
DEV void aload64(v2u& d, const unsigned char* sbase, int voff) { asm volatile("global_load_dwordx2 %0, %1, %2 sc1" : "=v"(d) : "v"(voff), "s"(sbase) : "memory"); }
DEV void aload64p(v2u& d, const void* sbase, int voff) { asm volatile("global_load_dwordx2 %0, %1, %2" : "=v"(d) : "v"(voff), "s"(sbase) : "memory"); }
#define GOT(n, reg) asm volatile("s_waitcnt vmcnt(%1)" : "+v"(reg) : "n"(n) : "memory")
DEV void dmaf(const unsigned char* ws, int blob, unsigned ldsb, int f, int l16) {
  unsigned keep, st; int vt;
  asm volatile("s_mov_b32 %0, m0\n\ts_lshl_b32 %1, %5, 10\n\ts_add_u32 m0, %1, %4\n\ts_add_u32 %1, %1, %7\n\tv_add_u32 %2, %1, %6\n\tglobal_load_lds_dwordx4 %2, %3 sc1\n\ts_mov_b32 m0, %0"
               : "=&s"(keep), "=&s"(st), "=&v"(vt) : "s"(ws), "s"(ldsb), "s"(__builtin_amdgcn_readfirstlane(f)), "v"(l16), "s"(__builtin_amdgcn_readfirstlane(blob)) : "memory", "scc");
}
DEV void dma_in(const void* base, int voff, unsigned ldsdst) {
  unsigned keep;
  asm volatile("s_mov_b32 %0, m0\n\ts_mov_b32 m0, %3\n\ts_nop 0\n\tglobal_load_lds_dwordx4 %1, %2\n\ts_mov_b32 m0, %0" : "=&s"(keep) : "v"(voff), "s"(base), "s"(ldsdst) : "memory");
}
template <int NW> DEV void dma_range(const unsigned char* ws, int blob, unsigned ldsbase, int f0, int f1, int w, int l16) {
  if (NW == 7 && w == 0) return;
#pragma unroll 1
  for (int f = f0 + (NW == 7 ? w - 1 : w); f < f1; f += NW) dmaf(ws, blob, ldsbase, f, l16);
}
DEV int poll_off(int lineA, int lineB, int lane) { return WS_FLAGS + ((lane & 16) ? lineB : lineA) * 128 + (lane & 15) * 8; }
DEV bool unwritten(v4u a, v4u b) { return __builtin_amdgcn_ballot_w64(max(max(a.x, a.z), max(b.x, b.z)) == SENT) != 0; }
DEV void wait_until(u64 t) { while ((long long)(__builtin_amdgcn_s_memrealtime() - t) < 0) __builtin_amdgcn_s_sleep(1); }
DEV void poll(const unsigned char* ws, int off, bool care, u64 tag) {
  for (unsigned spins = 0; spins < SPIN_CAP; ++spins) if (__all(!care || __hip_atomic_load((const GAS u64*)(ws + off), RLX_AGENT) == tag)) break;
  __builtin_amdgcn_fence(__ATOMIC_ACQUIRE, "wavefront");
}
DEV void set_flag(unsigned char* ws, int line, int idx, u64 tag) { __hip_atomic_store((GAS u64*)(ws + WS_FLAGS + line * 128) + idx, tag, RLX_AGENT); }
DEV int unit_row(int s, int i) {
  return s < 16 ? 36 * s + (i < 16 ? i : i + 4) : 36 * (8 * (s - 16) + 2 * ((i >> 2) & 3) + (i >> 4)) + 16 + (i & 3);
}
#define TS(i) do {} while (0)

template <bool SAFE> DEV bool phi_maps(const f32x4 (&pv)[2][2], int gq, v2u (&pl)[2], v2u (&ml)[2], v2u (&hq)[2]) {
  f32x4 ep[2], em[2]; float hp[2][2], hm[2][2], sp[2], sm[2];
#pragma unroll
  for (int h = 0; h < 2; ++h) {
    const f32x4 lo = pv[h][0], hi = pv[h][1];
    float mx, mn;
    if (SAFE) {
      mx = red_max(fmaxf(fmaxf(fmaxf(lo.x, lo.y), fmaxf(lo.z, lo.w)), fmaxf(hi.x, hi.y))) * LOG2E;
      mn = red_min(fminf(fminf(fminf(lo.x, lo.y), fminf(lo.z, lo.w)), fminf(hi.x, hi.y))) * LOG2E;
    } else mx = mn = hi.x * LOG2E;
#pragma unroll
    for (int r = 0; r < 4; ++r) { ep[h][r] = __builtin_amdgcn_exp2f(__builtin_fmaf(lo[r], LOG2E, -mx)); em[h][r] = __builtin_amdgcn_exp2f(-__builtin_fmaf(lo[r], LOG2E, -mn)); }
    hp[h][0] = SAFE ? __builtin_amdgcn_exp2f(__builtin_fmaf(hi.x, LOG2E, -mx)) : 1.0f;
    hm[h][0] = SAFE ? __builtin_amdgcn_exp2f(-__builtin_fmaf(hi.x, LOG2E, -mn)) : 1.0f;
    hp[h][1] = __builtin_amdgcn_exp2f(__builtin_fmaf(hi.y, LOG2E, -mx)); hm[h][1] = __builtin_amdgcn_exp2f(-__builtin_fmaf(hi.y, LOG2E, -mn));
    sp[h] = (ep[h].x + ep[h].y) + (ep[h].z + ep[h].w); sm[h] = (em[h].x + em[h].y) + (em[h].z + em[h].w);
  }
  auto a0 = __builtin_amdgcn_permlane16_swap(fu(sp[0]), fu(sm[0]), false, false);
  auto a1 = __builtin_amdgcn_permlane16_swap(fu(sp[1]), fu(sm[1]), false, false);
  auto a2 = __builtin_amdgcn_permlane32_swap(fu(uf(a0[0]) + uf(a0[1])), fu(uf(a1[0]) + uf(a1[1])), false, false);
  const float h0 = (gq & 1) ? hm[0][0] + hm[0][1] : hp[0][0] + hp[0][1], h1 = (gq & 1) ? hm[1][0] + hm[1][1] : hp[1][0] + hp[1][1];
  const float tot = (uf(a2[0]) + uf(a2[1])) + (gq < 2 ? h0 : h1);
  const float rc = __builtin_amdgcn_rcpf(tot);
  auto b0 = __builtin_amdgcn_permlane32_swap(fu(rc), fu(rc), false, false);
#pragma unroll
  for (int h = 0; h < 2; ++h) {
    auto b1 = __builtin_amdgcn_permlane16_swap(b0[h], b0[h], false, false);
    const float rp = uf(b1[0]), rm = uf(b1[1]);
    pl[h] = pk4(ep[h] * rp); ml[h] = pk4(em[h] * rm);
    hq[h] = v2u{pk2(hp[h][0] * rp, hp[h][1] * rp), pk2(hm[h][0] * rm, hm[h][1] * rm)};
  }
  return tot < BIG;
}
DEV void phi_pair(const f32x4 (&pv)[2][2], int gq, v2u (&pl)[2], v2u (&ml)[2], v2u (&hq)[2]) {
  if (__builtin_expect(!__all(phi_maps<false>(pv, gq, pl, ml, hq)), 0)) phi_maps<true>(pv, gq, pl, ml, hq);
}
constexpr int fr_s(int i) { return i >= 90 ? (i - 90) / 2 : i / 5; }
constexpr int fr_t(int i) { return i >= 90 ? 5 + (i - 90) % 2 : i % 5; }
__host__ __device__ constexpr int fr_slot(int i) { return i < 21 ? 55 + i : i < 52 ? i + 3 : i < 76 ? i - 52 : i; }
constexpr int xn(int ty, int u) { return u < 0 ? 6 : u < 8 ? 10 + 2 * u : ty == 1 ? 25 : u < 10 ? 25 : u < 17 ? 16 + u : 32; }
constexpr int on(int ty, int u) { return u < 0 ? 4 : ty == 1 ? (u < 3 ? 8 + 2 * u : 14) : (u < 5 ? 8 + 2 * u : 17); }
template <int lo, int hi, class F> DEV void steps(F&& f) {
  [&]<int... k>(std::integer_sequence<int, k...>) { (f(IC(lo + k)), ...); }(std::make_integer_sequence<int, hi - lo>{});
}

__global__ void __launch_bounds__(512, 2) fused_kernel(const u16* __restrict__ x0, const u16* __restrict__ wA, const u16* __restrict__ wphi, const u16* __restrict__ woA,
                                                       const u16* __restrict__ wB, const u16* __restrict__ woB, u16* __restrict__ out, unsigned char* ws0) {
  unsigned char* const ws = ws0;
  extern __shared__ __attribute__((aligned(16))) unsigned char smem[];
  const lptr L = (lptr)smem;
  const unsigned Lb = (unsigned)(uintptr_t)L;
  const int b = blockIdx.x, tid = threadIdx.x, lane0 = tid & 63, w0 = __builtin_amdgcn_readfirstlane(tid >> 6);
  const int p0 = ((b & 7) >> 1) * 4 + (b >> 6), g0 = (b & 1) * 8 + ((b >> 3) & 7);
  const rsrc_t rs = __builtin_amdgcn_make_buffer_rsrc((void*)ws, 0, WS_TOTAL, 0x00020000);
  TS(0);
  u64 tag;
  v4u xf[18];
  f32x4 res[3];
  v4u ring[15];
  float ssq;
  u64 t_go;
  u64 nret = 0;
  const f32x4 z = {0.f, 0.f, 0.f, 0.f};
  auto colsums7 = [&](int type) {
    int lane = lane0;
    asm volatile("" : "+v"(lane));
    const int base = WS_CSP + (type * 16 + g0) * 18 * 448 + (lane >> 5) * 9 * 448 + (lane & 31) * 16;
    v4u c[9];
#pragma unroll
    for (int i = 0; i < 9; ++i) c[i] = gld128(rs, (lane & 31) < 28 ? base + i * 448 : OOB);
    f32x4 t = __builtin_bit_cast(f32x4, c[0]);
#pragma unroll
    for (int i = 1; i < 9; ++i) t += __builtin_bit_cast(f32x4, c[i]);
#pragma unroll
    for (int r = 0; r < 4; ++r) { auto sw = __builtin_amdgcn_permlane32_swap(fu(t[r]), fu(t[r]), false, false); t[r] = uf(sw[0]) + uf(sw[1]); }
    if (lane < 28) *(LAS f32x4*)(L + L_CS + type * 448 + lane * 16) = t;
  };
  auto wq_off = [&](int s, int r) {
    const int pc = r == 0 ? tid : (tid + 512 > 863 ? 863 : tid + 512), i = pc / 27, q = pc - i * 27;
    return unit_row(s, i) * 3456 + (q / 9) * 1080 + q * 8 + g0 * 72;
  };
  auto wq_put = [&](int img, v2u d0, v2u d1) { sts64(L, img + tid * 8, d0); sts64(L, img + (tid + 512 > 863 ? 863 : tid + 512) * 8, d1); };
  auto wq_emit = [&](int img, int type, int s) {
    const int w = w0, lane = lane0, tok = lane & 15, gq = lane >> 4, trq = (lane >> 2) & 3, trp = lane & 3;
    if (w < 7) {
      const int wco = (w & 1) * 40 + (w < 2 ? 72 : w < 4 ? 144 : 0) - (w > 4 ? 40 : 0) + (w == 6 ? 80 : 0);
      int a0 = img + (4 * gq + trq) * 216 + (w == 4 ? trp * 72 + 32 : wco + trp * 8), a1 = a0 + 16 * 216;
      if (w == 4 && trp == 3) a0 = a1 = L_ONEQ;
      const int u = s < 16 ? s + 2 : s - 16, j = w >= 5 ? 90 + 2 * u + w - 5 : 5 * u + w;
      const v4u f = cat(ldstr(L, a0), ldstr(L, a1));
      unsigned one2 = 0x3F803F80u; asm volatile("" : "+v"(one2));
      gst128(rs, WS_WQ + (type * 16 + g0) * WQ_BLOB + fr_slot(j) * 1024 + lane * 16, f);
      const f32x4 cs = mfma(f, v4u{one2, one2, one2, one2}, z);
      if (tok == 0) gst128(rs, WS_CSP + ((type * 16 + g0) * 18 + s) * 448 + w * 64 + gq * 16, __builtin_bit_cast(v4u, cs));
    }
  };
  auto wo_off = [&](int s) { const int po = tid > 287 ? 287 : tid, io = po / 9; return unit_row(s, io) * 1152 + g0 * 72 + (po - io * 9) * 8; };
  auto wo_emit = [&](int img, int type, int s, int t) {
    const int lane = lane0, tok = lane & 15, gq = lane >> 4, trq = (lane >> 2) & 3, trp = lane & 3, g = g0;
    const int a0 = img + (4 * gq + trq) * 72 + (t == 2 ? 32 : t * 40 + trp * 8);
    const int idx = s < 16 ? 2 * (s & 7) + 2 + (s >> 3) : s - 16;
    const int o = WS_WO + (type * 8 + (g >> 1)) * WO_BLOB + (17 - idx) * 4608 + (t < 2 ? (g & 1) * 2048 + t * 1024 + lane * 16 : 4096 + (gq * 8 + (g & 1) * 4 + tok) * 16);
    gst128(rs, (t == 2 && tok >= 4) ? OOB : o, cat(ldstr(L, a0), ldstr(L, a0 + 16 * 72)));
  };
  {
    const int lane = lane0, tok = lane & 15, gq = lane >> 4, w = w0, p = p0, g = g0, T = 8 * p + w;
    constexpr int RAW_A = 147456;
    v2u dA[2], dE[2], r0, r1, r2, ep, dP;
    aload64p(dA[0], wA, wq_off(p, 0)); aload64p(dA[1], wA, wq_off(p, 1));
    aload64(ep, ws, WS_EPOCH);
    aload64p(dE[0], wA, wq_off(16 + (p & 1), 0)); aload64p(dE[1], wA, wq_off(16 + (p & 1), 1));
    PIN();
    auto x_dma = [&](auto i_) {
      constexpr int i = decltype(i_)::value;
      const int ci = i * 64 + lane, r = (ci * 911) >> 16;
      const int src = T * 18432 + ci * 16 - r * 16 + (ci - r * 72 < r ? 1152 : 0);
      dma_in(x0, src, Lb + w * 18432 + i * 1024);
    };
    steps<0, 6>(x_dma);
    if (tid == 0) sts64(L, L_ONEQ, v2u{0x3F80u, 0u});
    VMWAIT(6); PIN();
    asm volatile("" : "+v"(dA[0]), "+v"(dA[1]), "+v"(dE[0]), "+v"(dE[1]), "+v"(ep));
    wq_put(RAW_A, dA[0], dA[1]);
    tag = ((u64)(unsigned)__builtin_amdgcn_readfirstlane(ep.x) | ((u64)(unsigned)__builtin_amdgcn_readfirstlane(ep.y) << 32)) + 1;
    BAR();
    wq_emit(RAW_A, 0, p);
    if (p < 2) { BAR(); wq_put(RAW_A, dE[0], dE[1]); BAR(); wq_emit(RAW_A, 0, 16 + p); }
    PIN();
    steps<6, 12>(x_dma);
    VMWAIT(6);
    RAWBAR();
    if (tid == 448) set_flag(ws, FL_RT1 + g, p, tag);
    TS(1);
    steps<12, 18>(x_dma);
    const int xr = (16 * (8 * p + 4 * (g & 1) + (w & 3)) + tok) * 1152 + 72 * ((g & ~1) + (w >> 2));
    aload64p(r0, x0, xr + 8 * gq); aload64p(r1, x0, xr + 40 + 8 * gq); aload64p(r2, x0, xr + 32);
    const int phi_i = tid < 162 ? tid : 161;
    aload64p(dP, wphi, g * 1296 + phi_i * 8);
    int xo[36];
    {
      const int xb = w * 18432 + tok * 1152;
      auto xoff = [&](int byte) { int c = (byte >> 4) + tok; c -= c >= 72 ? 72 : 0; return xb + c * 16 + (byte & 8); };
#pragma unroll
      for (int s = 0; s < 16; ++s) { xo[2 * s] = xoff(72 * s + 8 * gq); xo[2 * s + 1] = xoff(72 * s + 40 + 8 * gq); }
#pragma unroll
      for (int s = 0; s < 4; ++s) xo[32 + s] = xoff(72 * (8 * (s >> 1) + 2 * gq + (s & 1)) + 32);
#pragma unroll
      for (int i = 0; i < 36; ++i) asm volatile("" : "+v"(xo[i]));
    }
    VMWAIT(4);
#pragma unroll
    for (int s = 0; s < 18; ++s) xf[s < 16 ? s + 2 : s - 16] = cat(lds64(L, xo[2 * s]), lds64(L, xo[2 * s + 1]));
    TS(2);
    if (w == 0) poll(ws, poll_off(FL_RT1 + g, FL_RT1 + g, lane), true, tag);
    asm volatile("s_waitcnt vmcnt(0)" : "+v"(r0), "+v"(r1), "+v"(r2), "+v"(dP) :: "memory");
    BAR();
    TS(3);
    if (w == 7) colsums7(0);
#pragma unroll 1
    for (int k = 0; k < 8; ++k) dmaf(ws, WS_WQ + g * WQ_BLOB, Lb, fr_slot(k * 8 + w), lane * 16);
    { const int d0 = 2 * phi_i, d1 = d0 + 1; *(LAS unsigned*)(L + L_PH + (d0 / 9) * 4 + d0 * 4) = dP.x; *(LAS unsigned*)(L + L_PH + (d1 / 9) * 4 + d1 * 4) = dP.y; }
    if (tid >= 448 && tid < 448 + 46) { const int i = tid - 448; *(LAS unsigned*)(L + L_PH + (i < 36 ? i * 40 + 36 : 1440 + (i - 36) * 4)) = 0u; }
    res[0] = unpk4(r0); res[1] = unpk4(r1); res[2] = unpk4(r2);
    {
      float q4[4] = {0.f, 0.f, 0.f, 0.f};
#pragma unroll
      for (int s = 0; s < 18; ++s)
#pragma unroll
        for (int j = 0; j < 4; ++j) q4[j] = dot2(xf[s][j], q4[j]);
      ssq = (q4[0] + q4[1]) + (q4[2] + q4[3]);
    }
    PIN();
    VMWAIT(3);
    BAR();
    TS(4);
#pragma unroll
    for (int i = 0; i < RING; ++i) ring[i] = lds128(L, fr_slot(i) * 1024 + lane * 16);
    PIN();
  }

  auto layer = [&](auto ty_, auto first_, const int it) __attribute__((always_inline)) {
    constexpr int ty = decltype(ty_)::value;
    constexpr bool first = decltype(first_)::value;
    const int Lr = 2 * it + ty;
    int lane = lane0, w = w0, p = p0, g = g0;
    unsigned char* ws = ws0;
    asm volatile("" : "+v"(lane), "+s"(w), "+s"(p), "+s"(g), "+s"(ws));
    const int T = 8 * p + w;
    const int tok = lane & 15, gq = lane >> 4, trq = (lane >> 2) & 3, trp = lane & 3, l16 = lane * 16;
    const bool last = (ty == 1) && (it == 1);
    int hi16 = l16 + 65536;
    asm volatile("" : "+v"(hi16));
    auto wfrag = [&](int slot) { return slot < 64 ? lds128(L, l16 + slot * 1024) : lds128(L, hi16 + (slot - 64) * 1024); };
    const int curW = WS_WQ + (ty * 16 + g) * WQ_BLOB, nxtW = WS_WQ + ((1 - ty) * 16 + g) * WQ_BLOB, curO = WS_WO + (ty * 8 + (g >> 1)) * WO_BLOB;
    const int flm = FL_HOP + Lr * 16;
    v4u rv[4];
    const int kw = w >> 2, To = 8 * p + 4 * (g & 1) + (w & 3), go = (g & ~1) + kw;
    auto mark = [&](int k, int base, bool on) {
      unsigned sv = SENT; asm volatile("" : "+v"(sv));
      gst128(rs, !on ? OOB : k == 0 ? base + l16 : gq == 0 ? base + tok * 16 : OOB, v4u{sv, sv, sv, sv});
    };
    auto mark_x = [&](int k, int Lx) { mark(k, WS_FX + Lx * FRAG_BUF + To * FT + (k ? 16384 + go * 256 : go * 1024), Lx < 3); };
    auto mark_o = [&](int k, int Lo) { mark(k, WS_FO + Lo * FRAG_BUF + T * FT + (k ? 16384 + g * 256 : g * 1024), Lo < 4); };
    auto got2 = [&](auto n_, v4u& a, v4u& b, auto&& again, int cat = 0) {
      asm volatile("s_waitcnt vmcnt(%2)" : "+v"(a), "+v"(b) : "n"(decltype(n_)::value) : "memory");
      if (__builtin_expect(unwritten(a, b), 0)) {
#pragma unroll 1
        for (unsigned spins = 0; spins < SPIN_CAP; ++spins) {
          __builtin_amdgcn_s_sleep(RETRY_SLEEP);
          again();
          asm volatile("s_waitcnt vmcnt(0)" : "+v"(a), "+v"(b) :: "memory");
          if (!unwritten(a, b)) break;
        }
      }
    };
    const int xtb = WS_FX + (Lr - 1) * FRAG_BUF + T * FT, xv0 = xtb + 4096 + l16, xv1 = xv0 + 8192, xvr = xtb + 16384 + gq * 512 + tok * 16;
    auto x_op = [&](auto k_) {
      constexpr int k = decltype(k_)::value;
      if constexpr (k < 4) aload<(k & 1) * 256 + (k >> 1) * 2048>(rv[k], ws, xvr);
      else if constexpr (k < 20) aload<((k - 4) % 8) * 1024 - 4096>(xf[k - 2], ws, k < 12 ? xv0 : xv1);
      else if constexpr (k < 25) dmaf(ws, curW, Lb, min(90 + (k - 20) * 8 + w, 125), l16);
      else if constexpr (k < 29) dmaf(ws, curO, Lb + L_O, min(O_LATE + (k - 25) * 8 + w, 80), l16);
      else dmaf(ws, nxtW, Lb, min(55 + (k - 29) * 8 + w, 75), l16);
    };
    v2u dB[2], dE[2], dO[3];
    auto l0_op = [&](auto k_) {
      constexpr int k = decltype(k_)::value;
      if constexpr (k < 8) dmaf(ws, curW, Lb, fr_slot(min((k + 8) * 8 + w, 125)), l16);
      else if constexpr (k < 10) aload64p(dB[k - 8], wB, wq_off(p, k - 8));
      else if constexpr (k < 12) aload64p(dE[k - 10], wB, wq_off(16 + (p & 1), k - 10));
      else if constexpr (k < 14) aload64p(dO[k - 12], k == 12 ? woA : woB, wo_off(p));
      else if constexpr (k == 14) aload64p(dO[2], p < 6 ? woA : woB, wo_off(16 + (p & 1)));
    };
    if (!first) { wait_until(t_go); steps<0, xn(ty, -1)>(x_op); }
    TS(8 + Lr * 12 + 10);
    PIN();
    f32x4 acc[7];
#pragma unroll
    for (int t = 0; t < 7; ++t) acc[t] = z;
    const int moff = first && lane >= 32 ? poll_off(FL_CP + p, FL_CP + p, lane) : poll_off(flm + g, flm + (first ? g ^ 1 : g), lane);
    constexpr int PI = 112;
    v2u pollv;
    constexpr int RAW_B = 36 * 1024, RAW_OA = RAW_B + 6912, RAW_OB = RAW_OA + 2304, RAW_E1 = RAW_OB + 2304;
    auto qkv_step = [&](auto i_) {
      constexpr int i = decltype(i_)::value, s = fr_s(i), t = fr_t(i);
      if (i < 90 && t == 0) {
        PIN();
        if (!first) {
          if (s == 0) {
            got2(IC(4), rv[0], rv[1], [&] { steps<0, 2>(x_op); });
            got2(IC(2), rv[2], rv[3], [&] { steps<2, 4>(x_op); });
            xf[0] = v4u{rv[0].x, rv[0].y, rv[1].x, rv[1].y}; xf[1] = v4u{rv[2].x, rv[2].y, rv[3].x, rv[3].y};
            ssq = (uf(rv[0].z) + uf(rv[1].z)) + (uf(rv[2].z) + uf(rv[3].z));
            PIN();
          }
          steps<xn(ty, s - 1), xn(ty, s)>(x_op);
          PIN();
          if (s == 7) RAWBAR();
          if (s >= 2 && s % 2 == 0) got2(IC(xn(ty, s) - s - 4), xf[s], xf[s + 1], [&] { steps<s + 2, s + 4>(x_op); });
        } else {
          if (s < 16) l0_op(IC(s));
          if (s == 5) { VMWAIT(6); RAWBAR(); }
          if (s == 10) { VMWAIT(3); RAWBAR(); }
          if (s >= 10 && s < 12) mark_o(s - 10, 0);
        }
        PIN();
      }
      if (i == 80 && ty == 0) RAWBAR();
      if (i == 85 && first) {
        PIN();
        asm volatile("s_waitcnt vmcnt(0)" : "+v"(dB[0]), "+v"(dB[1]), "+v"(dE[0]), "+v"(dE[1]), "+v"(dO[0]), "+v"(dO[1]), "+v"(dO[2]) :: "memory");
        const int po = (tid > 287 ? 287 : tid) * 8;
        wq_put(RAW_B, dB[0], dB[1]); sts64(L, RAW_OA + po, dO[0]); sts64(L, RAW_OB + po, dO[1]);
        if (p < 4) wq_put(RAW_E1, dE[0], dE[1]); else sts64(L, RAW_E1 + po, dO[2]);
        PIN();
      }
      if (i >= 92 && i < 108 && i % 4 == 0) { if (i < 100) mark_x((i - 92) / 4, Lr); else mark_o((i - 100) / 4, Lr + 1); PIN(); }
      if (i == PI && ty == 1) { PIN(); aload64(pollv, ws, moff); PIN(); }
      acc[t] = mfma(ring[i % RING], xf[s], acc[t]);
      if (i + RING < 126 && (i + RING < 90 || i >= 90)) ring[i % RING] = wfrag(fr_slot(i + RING));
      PINM();
    };
    const int csb = L_CS + ty * 448 + gq * 16;
    auto wph = [&](int h, int ft) {
      const int rl = L_PH + (20 * h + 4 * gq + trq) * 40, ru = L_PH + ((trq >> 1) == h ? (18 * h + (h ? 0 : 16) + (trq & 1)) * 40 : 1440);
      return cat(ldstr(L, rl + (ft ? 32 : trp * 8)), ldstr(L, ru + (ft ? 32 : trp * 8)));
    };
    const int vsw = gq * 128 + ((tok * 8) ^ ((gq >> 1) * 64));
    const int trl = trp * 128 + (((4 * gq + trq) * 8) ^ ((trp >> 1) * 64));
    v4u wp[2][2]; f32x4 cs[5];
    steps<0, 78>(qkv_step);
#pragma unroll
    for (int t = 0; t < 5; ++t) cs[t] = *(LAS f32x4*)(L + csb + t * 64);
    if (ty == 0) {
#pragma unroll
      for (int h = 0; h < 2; ++h) { wp[h][0] = wph(h, 0); wp[h][1] = wph(h, 1); }
    }
    PIN();
    steps<78, 90>(qkv_step);
    const float mean = hi_half(odd_rows(acc[4].x)) * (1.0f / 576.0f);
    const float var = red_add(ssq) * (1.0f / 576.0f) - mean * mean;
    const float rstd = __builtin_amdgcn_rsqf(fmaxf(var, 0.f) + 1e-5f);
    auto fix = [&](int t, float sc) { return (acc[t] - mean * (t < 5 ? cs[t < 5 ? t : 0] : *(LAS f32x4*)(L + csb + t * 64))) * sc; };
    v2u kq[2], vq[2];
    kq[0] = pk4(fix(0, rstd)); kq[1] = pk4(fix(1, rstd));
    const f32x4 remf = fix(4, rstd);
    const v2u remq = pk4(remf);
    const int vpos = gq * 256 + ((tok * 16) ^ ((gq >> 1) * 128));
    v4u kboth, vboth;
    const int kh = w >= 3 ? 1 : 0, kft = w - 3 * kh;
    if (ty == 0) {
      const v2u ku = gq == 1 ? remq : v2u{0u, 0u};
      f32x4 pv[2][2];
#pragma unroll
      for (int h = 0; h < 2; ++h) { const v4u bk = cat(kq[h], ku); pv[h][0] = mfma(wp[h][0], bk, z); pv[h][1] = mfma(wp[h][1], bk, z); }
      const int sb = L_STG + w * STG_REC;
      vq[0] = pk4(fix(2, rstd)); vq[1] = pk4(fix(3, rstd));
      sts64(L, sb + vsw, vq[0]); sts64(L, sb + 512 + vsw, vq[1]);
      if (gq == 2) sts64(L, sb + 3072 + tok * 8, remq);
      if (lane >= 62) sts64(L, sb + 3456 + (lane & 1) * 8, v2u{(lane & 1) ? 0u : 0x3F80u, 0u});
      PIN();
      v2u pl[2], ml[2], hq[2];
      phi_pair(pv, gq, pl, ml, hq);
#pragma unroll
      for (int h = 0; h < 2; ++h) {
        sts64(L, sb + 1024 + h * 1024 + vsw, pl[h]); sts64(L, sb + 1536 + h * 1024 + vsw, ml[h]);
        if (gq == 0) sts64(L, sb + 3200 + h * 128 + tok * 8, hq[h]);
      }
      TS(8 + Lr * 12 + 0);
      DRAIN();
      BAR();
      if (w < 6) {
        const int aa = L_STG + (kft < 2 ? 1024 + kh * 1024 + kft * 512 + trl : (trp == 0 ? 3200 + kh * 128 + (4 * gq + trq) * 8 : 3464));
        const int bl = L_STG + kh * 512 + trl, bh = L_STG + (trp == 0 ? 3072 + (4 * gq + trq) * 8 : 3456);
        v4u fa[4], fl[4], fh[4];
#pragma unroll
        for (int ks = 0; ks < 4; ++ks) {
          const int o0 = 2 * ks * STG_REC, o1 = o0 + STG_REC;
          fa[ks] = cat(ldstr(L, aa + o0), ldstr(L, aa + o1)); fl[ks] = cat(ldstr(L, bl + o0), ldstr(L, bl + o1)); fh[ks] = cat(ldstr(L, bh + o0), ldstr(L, bh + o1));
        }
        PIN();
        f32x4 dl = z, dh = z;
#pragma unroll
        for (int ks = 0; ks < 4; ++ks) { dl = mfma(fa[ks], fl[ks], dl); dh = mfma(fa[ks], fh[ks], dh); }
        const int rb = WS_KVP + ((it * 16 + g) * 16 + p) * KVP_REC + kh * 2880;
        const int slot = tok == 4 ? 2 : (tok >> 1) == kh ? (tok & 1) : -1;
        const bool rowok = kft < 2 || gq == 0;
        if (rowok) gst128(rs, rb + kft * 1024 + l16, __builtin_bit_cast(v4u, dl));
        if (rowok && slot >= 0) gst128(rs, rb + 2304 + kft * 256 + (gq * 4 + slot) * 16, __builtin_bit_cast(v4u, dh));
      } else {
#pragma unroll
        for (int j = 0; j < 4; ++j) { unsigned zz = 0u; asm volatile("" : "+v"(zz)); sts128(L, (w - 6) * 4096 + j * 1024 + l16, v4u{zz, zz, zz, zz}); }
      }
      if (first) {
        wq_emit(RAW_B, 1, p);
        if (p >= 2 && p < 4) wq_emit(RAW_E1, 1, 16 + (p & 1));
        if (w < 3) wo_emit(RAW_OA, 0, p, w); else if (w < 6) wo_emit(RAW_OB, 1, p, w - 3);
        if (p >= 4 && p < 8 && w < 3) wo_emit(RAW_E1, p < 6 ? 0 : 1, 16 + (p & 1), w);
      }
    } else {
      const int rb = WS_KVX + it * KVX_BUF + (T * 16 + g) * KVX_REC;
      gst128(rs, rb + l16, kboth = (gq & 1) ? cat(kq[1], kq[0]) : cat(kq[0], kq[1]));
      if (gq == 1 || gq == 2) gst64(rs, rb + 2048 + (gq - 1) * 128 + tok * 8, remq);
      vq[0] = pk4(fix(2, rstd)); vq[1] = pk4(fix(3, rstd));
      TS(8 + Lr * 12 + 0);
      gst128(rs, rb + 1024 + vpos, vboth = (gq & 1) ? cat(vq[1], vq[0]) : cat(vq[0], vq[1]));
      VMWAIT(3);
      RAWBAR();
      const int ib = (16 + w) * IMG_REC;
      sts128(L, ib + l16, kboth); sts128(L, ib + 1024 + vpos, vboth);
      if (gq == 1 || gq == 2) sts64(L, ib + 2048 + (gq - 1) * 128 + tok * 8, remq);
      if (lane < 3) { const int i = w + 8 * lane; sts128(L, i * IMG_REC + 2304, v4u{0x3F80u, 0u, (8 * (p - 2) + i >= 0) ? 0u : 0xF149u, 0u}); }
    }
    PIN();
#pragma unroll
    for (int j = 90; j < 90 + RING; ++j) ring[j % RING] = wfrag(fr_slot(j));
    PIN();
    DRAIN();
    RAWBAR();
    if (tid == 448) { set_flag(ws, flm + g, p, tag); if (first) set_flag(ws, FL_CP + p, g, tag); }
    TS(8 + Lr * 12 + 1);
    PIN();
    steps<90, 126>(qkv_step);
    auto poll_done = [&](bool care) {
      DRAIN(); PIN();
      asm volatile("" : "+v"(pollv));
      if (!__all(!care || ((u64)pollv.y << 32 | pollv.x) == tag)) poll(ws, moff, care, tag);
    };
    TS(8 + Lr * 12 + 2);
    v4u dp[16];
    const int pi = w * 64 + lane;
    if (ty == 1) {
      poll_done(tok == p - 1 || tok == p - 2);
#pragma unroll
      for (int j = 0; j < 2; ++j) {
        const int Tg = 8 * (p - 2) + w + 8 * j, ib = (w + 8 * j) * IMG_REC;
        if (Tg >= 0) {
          const int rb = WS_KVX + it * KVX_BUF + (Tg * 16 + g) * KVX_REC;
          dmaf(ws, rb, Lb + ib, 0, l16); dmaf(ws, rb, Lb + ib, 1, l16);
          if (lane < 16) dmaf(ws, rb, Lb + ib, 2, l16);
        } else {
          unsigned zz = 0u; asm volatile("" : "+v"(zz));
          sts128(L, ib + l16, v4u{zz, zz, zz, zz}); sts128(L, ib + 1024 + l16, v4u{zz, zz, zz, zz});
          if (lane < 16) sts128(L, ib + 2048 + l16, v4u{zz, zz, zz, zz});
        }
      }
    }
    PIN();
    if (ty == 1) TS(8 + Lr * 12 + 3);
    constexpr float QS = 0.23570226039551584f * LOG2E;
    const v2u q0 = pk4(fix(5, ty == 1 ? rstd * QS : rstd)), q1 = pk4(fix(6, ty == 1 ? rstd * QS : rstd));
    v2u olo[2]; unsigned ohi[2];
    if (ty == 0) {
      v4u qf[2][2];
      {
        const v2u qu = gq == 0 ? remq : v2u{0u, 0u};
        f32x4 pv[2][2];
#pragma unroll
        for (int h = 0; h < 2; ++h) { const v4u bq = cat(h ? q1 : q0, qu); pv[h][0] = mfma(wp[h][0], bq, z); pv[h][1] = mfma(wp[h][1], bq, z); }
        PIN();
        if (w == 0) aload64(pollv, ws, moff);
        PIN();
        v2u pl[2], ml[2], hq[2];
        phi_pair(pv, gq, pl, ml, hq);
#pragma unroll
        for (int h = 0; h < 2; ++h) { qf[h][0] = cat(pl[h], ml[h]); qf[h][1] = cat(hq[h], v2u{0u, 0u}); }
      }
      PIN();
      if (w == 0) poll_done(true);
      RAWBAR();
      TS(8 + Lr * 12 + 3);
      if (w >= 6) {
        __builtin_amdgcn_s_sleep(18);
        DRAIN();
#pragma unroll 1
        for (int f = (first ? O_LATE : 0) + w - 6; f < (first ? 81 : O_LATE); f += 2) dmaf(ws, curO, Lb + L_O, f, l16);
      } else {
        const int po = pi < 360 ? WS_KVP + (it * 16 + g) * 16 * KVP_REC + pi * 16 : OOB;
#pragma unroll
        for (int q = 0; q < 16; ++q) dp[q] = gld128(rs, po, q * KVP_REC);
        PIN();
        f32x4 s0 = __builtin_bit_cast(f32x4, dp[0]), s1 = __builtin_bit_cast(f32x4, dp[1]);
#pragma unroll
        for (int q = 2; q < 16; q += 2) { s0 += __builtin_bit_cast(f32x4, dp[q]); s1 += __builtin_bit_cast(f32x4, dp[q + 1]); }
        const v2u sv = pk4(s0 + s1);
        const int h = pi >= 180 ? 1 : 0, j = pi - 180 * h;
        const int e = j - 144, sl = e & 3, half = j < 144 ? (j >> 6) & 1 : (e >> 4) & 1;
        const int blk = j < 128 ? 0 : j < 144 ? 1024 : e < 32 ? 2048 : 3072;
        const int ln = j < 144 ? (j & 63) : ((e >> 2) & 3) * 16 + (sl < 2 ? 2 * h + sl : 4);
        const int dst = h * 4096 + blk + ln * 16 + half * 8;
        if (pi < 360 && (j < 144 || sl < 3)) sts64(L, dst, sv);
        if (pi < 360 && j >= 144 && sl == 2) sts64(L, dst + 128, sv);
      }
      BAR();
      TS(8 + Lr * 12 + 4);
      v4u kf[2][4];
#pragma unroll
      for (int h = 0; h < 2; ++h)
#pragma unroll
        for (int j = 0; j < 4; ++j) kf[h][j] = lds128(L, h * 4096 + j * 1024 + l16);
      PIN();
#pragma unroll
      for (int h = 0; h < 2; ++h) {
        const f32x4 dl2 = mfma(kf[h][1], qf[h][1], mfma(kf[h][0], qf[h][0], z));
        const f32x4 dh2 = mfma(kf[h][3], qf[h][1], mfma(kf[h][2], qf[h][0], z));
        const float r = __builtin_amdgcn_rcpf(odd_rows(dh2.x) + 1e-6f);
        olo[h] = pk4(dl2 * r); ohi[h] = h == 0 ? pk2(dh2.x * r, dh2.y * r) : pk2(dh2.z * r, dh2.w * r);
      }
    } else {
      v4u qf[2];
      {
        const v2u qr = pk4(remf * QS);
        const unsigned bias1 = gq == 1 ? 0x3F80u : 0u;
        qf[0] = cat(q0, v2u{gq == 0 ? qr.x : bias1, 0u});
        qf[1] = cat(q1, v2u{bias1, gq == 0 ? qr.y : 0u});
      }
      const int wb = w * IMG_REC;
      const int kuo = wb + (gq == 1 ? 2312 : 2048 + tok * 8);
      auto kfrag = [&](int h, int i) { return cat(lds64(L, wb + ((h ^ (gq & 1)) * 8) + l16 + i * IMG_REC), lds64(L, kuo + i * IMG_REC)); };
      float nm[2];
#pragma unroll
      for (int h = 0; h < 2; ++h) {
        const f32x4 sd = mfma(cat(kq[h], gq == 1 ? v2u{0u, 0u} : lds64(L, kuo + 16 * IMG_REC)), qf[h], z);
        float m = (4 * gq <= tok) ? sd.x : -3e38f;
#pragma unroll
        for (int r = 1; r < 4; ++r) m = fmaxf(m, (4 * gq + r <= tok) ? sd[r] : -3e38f);
        nm[h] = -red_max(m);
      }
      const int vl = wb + 1024 + trp * 256 + (((4 * gq + trq) * 16) ^ ((trp >> 1) * 128));
      const int vh = wb + (trp == 0 ? 2176 + (4 * gq + trq) * 8 : 2304);
      const unsigned m0 = (4 * gq + 0 <= tok ? 0xffffu : 0u) | (4 * gq + 1 <= tok ? 0xffff0000u : 0u);
      const unsigned m1 = (4 * gq + 2 <= tok ? 0xffffu : 0u) | (4 * gq + 3 <= tok ? 0xffff0000u : 0u);
      PIN();
      DRAIN();
      BAR();
      TS(8 + Lr * 12 + 4);
#pragma unroll 1
      for (int pass = 0; pass < 2; ++pass) {
        v4u kf[34]; f32x4 sc[2][17]; v4u va, vb, van, vbn; f32x4 dl, dh;
        bool ok = true;
        constexpr int ord[17] = {16, 0, 1, 2, 3, 4, 5, 6, 7, 8, 9, 10, 11, 12, 13, 14, 15};
#pragma unroll
        for (int j = 0; j < 8; ++j) kf[j] = kfrag(0, ord[j]);
        auto vread = [&](int h, int ks) {
          const int i0 = ks == 0 ? 0 : 2 * ks - 1, i1 = ks == 0 ? 16 : ks < 8 ? 2 * ks : 15, vo = vl + ((h ^ (trp & 1)) * 8);
          van = cat(ldstr(L, vo + i0 * IMG_REC), ldstr(L, vo + i1 * IMG_REC));
          vbn = cat(ldstr(L, vh + i0 * IMG_REC), ldstr(L, vh + i1 * IMG_REC));
        };
        auto qk = [&](int j) { const int h = j / 17; sc[h][ord[j % 17]] = mfma(kf[j], qf[h], f32x4{nm[h], nm[h], nm[h], nm[h]}); };
        auto pexp = [&](f32x4 x) {
          f32x4 e;
#pragma unroll
          for (int r = 0; r < 4; ++r) e[r] = __builtin_amdgcn_exp2f(x[r]);
          return pk4(e);
        };
        vread(0, 0);
        PIN();
#pragma unroll
        for (int j = 0; j < 4; ++j) qk(j);
        PIN();
        auto att_step = [&](auto n_) {
          constexpr int n = decltype(n_)::value, h = n / 9, ks = n % 9;
          va = van; vb = vbn;
          if (n < 17) vread((n + 1) / 9, (n + 1) % 9);
          if (2 * n + 8 < 34) kf[2 * n + 8] = kfrag((2 * n + 8) / 17, ord[(2 * n + 8) % 17]);
          if (2 * n + 9 < 34) kf[2 * n + 9] = kfrag((2 * n + 9) / 17, ord[(2 * n + 9) % 17]);
          if (2 * n + 4 < 34) qk(2 * n + 4);
          if (2 * n + 5 < 34) qk(2 * n + 5);
          v4u bp;
          if (ks == 0) {
            f32x4 mg;
#pragma unroll
            for (int r = 0; r < 4; ++r) mg[r] = (4 * gq + r <= tok) ? sc[h][16][r] : sc[h][0][r];
            const v2u p0 = pexp(mg);
            bp = v4u{p0.x & ~m0, p0.y & ~m1, p0.x & m0, p0.y & m1};
          } else bp = cat(pexp(sc[h][2 * ks - 1]), ks < 8 ? pexp(sc[h][ks < 8 ? 2 * ks : 0]) : v2u{0u, 0u});
          dl = mfma(va, bp, ks == 0 ? z : dl); dh = mfma(vb, bp, ks == 0 ? z : dh);
          int wo_ = w; asm volatile("" : "+s"(wo_));
          if (n < 11) dmaf(ws, curO, Lb + L_O, min(n * 8 + wo_, 80), l16);
          else if (n < 14 && !last) dmaf(ws, nxtW, Lb, min(55 + (n - 11) * 8 + wo_, 75), l16);
          if (ks == 8) {
            const float l = odd_rows(dh.x);
            ok = ok && l < BIG;
            const float r = __builtin_amdgcn_rcpf(l);
            olo[h] = pk4(dl * r); ohi[h] = h == 0 ? pk2(dh.x * r, dh.y * r) : pk2(dh.z * r, dh.w * r);
          }
          PIN();
        };
        steps<0, 18>(att_step);
        if (__builtin_expect(__all(ok), 1)) break;
#pragma unroll
        for (int h = 0; h < 2; ++h) {
          const f32x4 s0 = mfma(kfrag(h, 0), qf[h], z), s16 = mfma(kfrag(h, 16), qf[h], z);
          float m = -3e38f;
#pragma unroll
          for (int r = 0; r < 4; ++r) m = fmaxf(m, (4 * gq + r <= tok) ? s16[r] : s0[r]);
#pragma unroll 1
          for (int i = 1; i < 16; ++i) { const f32x4 s = mfma(kfrag(h, i), qf[h], z); m = fmaxf(fmaxf(fmaxf(m, s.x), fmaxf(s.y, s.z)), s.w); }
          nm[h] = -red_max(m);
        }
      }
    }
    TS(8 + Lr * 12 + 5);
    gst128(rs, WS_FO + Lr * FRAG_BUF + T * FT + g * 1024 + l16, cat(olo[0], olo[1]));
    gst128(rs, gq == 0 ? WS_FO + Lr * FRAG_BUF + T * FT + 16384 + g * 256 + tok * 16 : OOB, v4u{ohi[0], ohi[1], 0u, 0u});
    int omine = L_O + (1 - kw) * 4608 + kw * 2048 + l16, oother = L_O + (1 - kw) * 4608 + (1 - kw) * 2048 + l16, orem = L_O + (1 - kw) * 4608 + 4096 + (gq * 8 + (tok & 7)) * 16;
    asm volatile("" : "+v"(omine), "+v"(oother), "+v"(orem));
    auto ofrag = [&](int i) { const int u = i / 5, t = i % 5; return lds128(L, (8 - u) * 9216 + (t < 2 ? omine + t * 1024 : t < 4 ? oother + (t - 2) * 1024 : orem)); };
    VMWAIT(2);
    RAWBAR();
    t_go = __builtin_amdgcn_s_memrealtime() + ((ty == 1 && !(g & 1)) ? 0 : HOP_O);
    TS(8 + Lr * 12 + 6);
    if (first) dma_range<8>(ws, curO, Lb + L_O, 0, O_LATE, w, l16);
    if (first && w == 7) colsums7(1);
#pragma unroll
    for (int i = 0; i < 15; ++i) ring[i] = ofrag(i);
    PIN();
    if (first) { DRAIN(); RAWBAR(); }
    wait_until(t_go);
    TS(8 + Lr * 12 + 7);
    const int ob = WS_FO + Lr * FRAG_BUF + To * FT, ov0 = ob + kw * 8192 + 4096 + l16, ovr = ob + 16384 + kw * 2048 + gq * 512 + tok * 16;
    auto o_op = [&](auto k_) {
      constexpr int k = decltype(k_)::value;
      if constexpr (k < 2) aload<k * 256>(rv[k], ws, ovr);
      else if constexpr (k < 10) aload<(k - 2) * 1024 - 4096>(xf[k - 1], ws, ov0);
      else if constexpr (k < 14) dmaf(ws, nxtW, Lb, min(24 + (k - 10) * 8 + w, 54), l16);
      else dmaf(ws, nxtW, Lb, min(55 + (k - 14) * 8 + w, 75), l16);
    };
    steps<0, on(first ? 0 : 1, -1)>(o_op);
    PIN();
    f32x4 oa[5] = {res[0], res[1], z, z, z};
    auto wo_step = [&](auto i_) {
      constexpr int i = decltype(i_)::value, u = i / 5, t = i % 5;
      if (t == 0) {
        PIN();
        if (u == 0) {
          got2(IC(2), rv[0], rv[1], [&] { steps<0, 2>(o_op); }, 1);
          xf[0] = v4u{rv[0].x, rv[0].y, rv[1].x, rv[1].y};
          PIN();
        }
        steps<on(first ? 0 : 1, u - 1), on(first ? 0 : 1, u)>(o_op);
        PIN();
        if (u % 2 == 1) got2(IC(on(first ? 0 : 1, u) - u - 3), xf[u], xf[u + 1], [&] { steps<u + 1, u + 3>(o_op); }, 1);
        PIN();
      }
      oa[t] = mfma(ring[i % 15], xf[u], oa[t]);
      if (i + 15 < 45) ring[i % 15] = ofrag(i + 15);
      PINM();
    };
    steps<0, 45>(wo_step);
    {
      const int xm = L_XCH + w * 3072 + l16, xo = L_XCH + (w ^ 4) * 3072 + l16;
      sts128(L, xm, __builtin_bit_cast(v4u, oa[2])); sts128(L, xm + 1024, __builtin_bit_cast(v4u, oa[3])); sts128(L, xm + 2048, __builtin_bit_cast(v4u, oa[4]));
      BAR();
      res[0] = oa[0] + __builtin_bit_cast(f32x4, lds128(L, xo)); res[1] = oa[1] + __builtin_bit_cast(f32x4, lds128(L, xo + 1024));
      f32x4 rm = oa[4] + __builtin_bit_cast(f32x4, lds128(L, xo + 2048));
#pragma unroll
      for (int r = 0; r < 4; ++r) { auto sw = __builtin_amdgcn_permlane16_swap(fu(rm[r]), fu(rm[r]), false, false); rm[r] = uf(kw ? sw[1] : sw[0]); }
      res[2] += rm;
    }
    TS(8 + Lr * 12 + 8);
    const v2u xa = pk4(res[0]), xb = pk4(res[1]), xc = pk4(res[2]);
    if (!last) {
      const float sq = red_add(dot2(xa.x, dot2(xa.y, 0.f)) + dot2(xb.x, dot2(xb.y, 0.f))) + dot2(xc.x, dot2(xc.y, 0.f));
      gst128(rs, WS_FX + Lr * FRAG_BUF + To * FT + go * 1024 + l16, cat(xa, xb));
      gst128(rs, gq == 0 ? WS_FX + Lr * FRAG_BUF + To * FT + 16384 + go * 256 + tok * 16 : OOB, v4u{xc.x, xc.y, fu(sq), 0u});
      VMWAIT(2);
      RAWBAR();
      t_go = __builtin_amdgcn_s_memrealtime() + HOP_X + (ty == 1 ? (kw - (g & 1)) * HOP_O : 0);
      TS(8 + Lr * 12 + 9);
      dma_range<8>(ws, nxtW, Lb, 0, 24, w, l16);
      dma_range<8>(ws, nxtW, Lb, 76, 90, w, l16);
#pragma unroll
      for (int i = 0; i < RING; ++i) ring[i] = wfrag(fr_slot(i));
      PIN();
    } else {
      GAS unsigned char* orow = (GAS unsigned char*)out + (16 * To + tok) * 1152 + 72 * go;
      *(GAS v2u*)(orow + 8 * gq) = xa;
      *(GAS v2u*)(orow + 40 + 8 * gq) = xb;
      if (gq == 0) *(GAS v2u*)(orow + 32) = xc;
      if (b == 0 && tid == 0) __hip_atomic_store((GAS u64*)(ws + WS_EPOCH), tag, RLX_AGENT);
      DRAIN();
      TS(8 + Lr * 12 + 9);
    }
  };
  layer(IC(0), IC(1), 0);
#pragma unroll 1
  for (int it = 0; it < 2; ++it) {
    layer(IC(1), IC(0), it);
    if (it == 0) layer(IC(0), IC(0), 1);
  }
}

extern "C" void kernel_launch(void* const* d_in, const int* in_sizes, int n_in, void* d_out, int out_size, void* d_ws, size_t ws_size, hipStream_t stream) {
  (void)hipFuncSetAttribute((const void*)fused_kernel, hipFuncAttributeMaxDynamicSharedMemorySize, L_TOTAL);
  fused_kernel<<<256, 512, L_TOTAL, stream>>>((const u16*)d_in[0], (const u16*)d_in[1], (const u16*)d_in[2], (const u16*)d_in[3],
                                              (const u16*)d_in[5], (const u16*)d_in[6], (u16*)d_out, (unsigned char*)d_ws);
}
```

```cpp
#include <hip/hip_runtime.h>
#include <type_traits>
#include <utility>
typedef unsigned short u16;
typedef unsigned long long u64;
typedef unsigned v4u __attribute__((ext_vector_type(4)));
typedef unsigned v2u __attribute__((ext_vector_type(2)));
typedef float f32x4 __attribute__((ext_vector_type(4)));
typedef float f32x2 __attribute__((ext_vector_type(2)));
typedef short bf16x8 __attribute__((ext_vector_type(8)));
typedef short s16x4 __attribute__((ext_vector_type(4)));
typedef __bf16 bf16x2 __attribute__((ext_vector_type(2)));
#define GAS __attribute__((address_space(1)))
#define LAS __attribute__((address_space(3)))
#define RLX_AGENT __ATOMIC_RELAXED, __HIP_MEMORY_SCOPE_AGENT
#define DEV __device__ __forceinline__
#define IC(n) std::integral_constant<int, (n)>{}

constexpr int WS_EPOCH = 0;
constexpr int WS_FLAGS = 4096;
constexpr int FL_RT1 = 0, FL_CP = 16, FL_HOP = 32;
constexpr int WQ_BLOB = 129024, WO_BLOB = 41984;
constexpr int WS_WQ = 1 << 20;
constexpr int WS_WO = WS_WQ + 2 * 16 * WQ_BLOB;
constexpr int FT = 20480;
constexpr int FRAG_BUF = 128 * FT;
constexpr int WS_FX = 8 << 20;
constexpr int WS_FO = WS_FX + 3 * FRAG_BUF;
constexpr int KVX_REC = 2304;
constexpr int KVX_BUF = 128 * 16 * KVX_REC;
constexpr int WS_KVX = WS_FO + 4 * FRAG_BUF;
constexpr int KVP_REC = 3072;
constexpr int WS_KVP = WS_KVX + 2 * KVX_BUF;
constexpr int WS_CSP = WS_KVP + 2 * 256 * KVP_REC;
constexpr int WS_TOTAL = WS_CSP + 2 * 16 * 18 * 448;
static_assert(WS_WO + 2 * 16 * WO_BLOB <= WS_FX, "ws map");
constexpr int L_O = 119040, L_TOTAL = 163840;
constexpr int O_LATE = 10, Q_LATE = 116;
constexpr int L_STG = 8192;
constexpr int RAW_B = WQ_BLOB, RAW_OA = RAW_B + 6912, RAW_OB = RAW_OA + 2304, RAW_E1 = RAW_OB + 2304;
constexpr int IMG_REC = 2320, STG_REC = 3472;
constexpr int L_CS = L_O + WO_BLOB;
constexpr int L_PH = L_CS + 896;
constexpr int L_ONEQ = L_TOTAL - 16;
constexpr int L_SYN = L_TOTAL - 32;
static_assert(L_PH + 1480 <= L_SYN && L_PH % 8 == 0 && L_O + O_LATE * 1024 >= WQ_BLOB && L_O >= Q_LATE * 1024 && 24 * IMG_REC <= 55 * 1024, "lds map");
constexpr int OOB = 0x7ffffff0;
constexpr unsigned SPIN_CAP = 1u << 16;
constexpr float LOG2E = 1.4426950408889634f;
constexpr float BIG = 18446744073709551616.0f;
constexpr int RING = 14;
constexpr int QP = 106;
constexpr int FI = 108;
constexpr int HOP_O = 90, HOP_X = 90, HOP_M = 20, HOP_P = 100;
constexpr int RETRY_SLEEP = 2, SLP67 = 10, UPX = 8, UPO = 12, XB1 = 6, XB2 = 12, CODEPF = 26;
constexpr unsigned SENT = 0xFFFFFFFFu;

DEV f32x4 mfma(v4u a, v4u b, f32x4 c) {
  return __builtin_amdgcn_mfma_f32_16x16x32_bf16(__builtin_bit_cast(bf16x8, a), __builtin_bit_cast(bf16x8, b), c, 0, 0, 0);
}
DEV unsigned pk2(float lo, float hi) { f32x2 v = {lo, hi}; return __builtin_bit_cast(unsigned, __builtin_convertvector(v, bf16x2)); }
DEV v2u pk4(f32x4 v) { return v2u{pk2(v.x, v.y), pk2(v.z, v.w)}; }
DEV unsigned fu(float f) { return __builtin_bit_cast(unsigned, f); }
DEV float uf(unsigned u) { return __builtin_bit_cast(float, u); }
DEV f32x4 unpk4(v2u u) { return f32x4{uf(u.x << 16), uf(u.x & 0xffff0000u), uf(u.y << 16), uf(u.y & 0xffff0000u)}; }
DEV float dot2(unsigned a, float c) { return __builtin_amdgcn_fdot2_f32_bf16(__builtin_bit_cast(bf16x2, a), __builtin_bit_cast(bf16x2, a), c, false); }
DEV v4u cat(v2u a, v2u b) { return v4u{a.x, a.y, b.x, b.y}; }
DEV float red_add(float x) { auto a = __builtin_amdgcn_permlane16_swap(fu(x), fu(x), false, false); x = uf(a[0]) + uf(a[1]); auto b = __builtin_amdgcn_permlane32_swap(fu(x), fu(x), false, false); return uf(b[0]) + uf(b[1]); }
DEV float red_max(float x) { auto a = __builtin_amdgcn_permlane16_swap(fu(x), fu(x), false, false); x = fmaxf(uf(a[0]), uf(a[1])); auto b = __builtin_amdgcn_permlane32_swap(fu(x), fu(x), false, false); return fmaxf(uf(b[0]), uf(b[1])); }
DEV float red_min(float x) { auto a = __builtin_amdgcn_permlane16_swap(fu(x), fu(x), false, false); x = fminf(uf(a[0]), uf(a[1])); auto b = __builtin_amdgcn_permlane32_swap(fu(x), fu(x), false, false); return fminf(uf(b[0]), uf(b[1])); }
DEV float odd_rows(float x) { auto a = __builtin_amdgcn_permlane16_swap(fu(x), fu(x), false, false); return uf(a[1]); }
DEV float hi_half(float x) { auto a = __builtin_amdgcn_permlane32_swap(fu(x), fu(x), false, false); return uf(a[1]); }

typedef LAS unsigned char* lptr;
DEV v4u lds128(lptr L, int off) { return *(LAS v4u*)(L + off); }
DEV v2u lds64(lptr L, int off) { return *(LAS v2u*)(L + off); }
DEV v2u ldstr(lptr L, int off) { return __builtin_bit_cast(v2u, __builtin_amdgcn_ds_read_tr16_b64_v4i16((LAS s16x4*)(L + off))); }
DEV void sts128(lptr L, int off, v4u v) { *(LAS v4u*)(L + off) = v; }
DEV void sts64(lptr L, int off, v2u v) { *(LAS v2u*)(L + off) = v; }
#define BAR() do { asm volatile("s_waitcnt lgkmcnt(0)" ::: "memory"); __builtin_amdgcn_s_barrier(); asm volatile("" ::: "memory"); } while (0)
#define RAWBAR() do { asm volatile("" ::: "memory"); __builtin_amdgcn_s_barrier(); asm volatile("" ::: "memory"); } while (0)
#define VMWAIT(n) asm volatile("s_waitcnt vmcnt(%0)" :: "n"(n) : "memory")
#define DRAIN() VMWAIT(0)
#define PIN() __builtin_amdgcn_sched_barrier(0)
#define PINM() __builtin_amdgcn_sched_barrier(0x406)
typedef __amdgpu_buffer_rsrc_t rsrc_t;
DEV v4u gld128(rsrc_t r, int off, int soff = 0) { return __builtin_amdgcn_raw_buffer_load_b128(r, off, soff, 16); }
DEV void gst128(rsrc_t r, int off, v4u v) { __builtin_amdgcn_raw_buffer_store_b128(v, r, off, 0, 16); }
DEV void gst64(rsrc_t r, int off, v2u v) { __builtin_amdgcn_raw_buffer_store_b64(v, r, off, 0, 16); }
template <int OFF> DEV void aload(v4u& d, const unsigned char* sbase, int voff) { asm volatile("global_load_dwordx4 %0, %1, %2 offset:%3 sc1" : "=v"(d) : "v"(voff), "s"(sbase), "n"(OFF) : "memory"); }
DEV void aload64(v2u& d, const unsigned char* sbase, int voff) { asm volatile("global_load_dwordx2 %0, %1, %2 sc1" : "=v"(d) : "v"(voff), "s"(sbase) : "memory"); }
DEV void aload64p(v2u& d, const void* sbase, int voff) { asm volatile("global_load_dwordx2 %0, %1, %2" : "=v"(d) : "v"(voff), "s"(sbase) : "memory"); }
DEV void aloadb(v4u& d, rsrc_t r, int voff, int soff) { asm volatile("buffer_load_dwordx4 %0, %1, %2, %3 offen sc1" : "=v"(d) : "v"(voff), "s"(r), "s"(soff) : "memory"); }
DEV void dmaf(const unsigned char* ws, int blob, unsigned ldsb, int f, int l16) {
  unsigned keep, st; int vt;
  asm volatile("s_mov_b32 %0, m0\n\ts_lshl_b32 %1, %5, 10\n\ts_add_u32 m0, %1, %4\n\ts_add_u32 %1, %1, %7\n\tv_add_u32 %2, %1, %6\n\tglobal_load_lds_dwordx4 %2, %3 sc1\n\ts_mov_b32 m0, %0"
               : "=&s"(keep), "=&s"(st), "=&v"(vt) : "s"(ws), "s"(ldsb), "s"(__builtin_amdgcn_readfirstlane(f)), "v"(l16), "s"(__builtin_amdgcn_readfirstlane(blob)) : "memory", "scc");
}
DEV void dma_in(const void* base, int voff, unsigned ldsdst) {
  unsigned keep;
  asm volatile("s_mov_b32 %0, m0\n\ts_mov_b32 m0, %3\n\ts_nop 0\n\tglobal_load_lds_dwordx4 %1, %2\n\ts_mov_b32 m0, %0" : "=&s"(keep) : "v"(voff), "s"(base), "s"(ldsdst) : "memory");
}
template <int NW> DEV void dma_range(const unsigned char* ws, int blob, unsigned ldsbase, int f0, int f1, int w, int l16) {
  if (NW == 7 && w == 0) return;
#pragma unroll 1
  for (int f = f0 + (NW == 7 ? w - 1 : w); f < f1; f += NW) dmaf(ws, blob, ldsbase, f, l16);
}
DEV int poll_off(int lineA, int lineB, int lane) { return WS_FLAGS + ((lane & 16) ? lineB : lineA) * 128 + (lane & 15) * 8; }
DEV bool unwritten(v4u a, v4u b) { return __builtin_amdgcn_ballot_w64(max(max(a.x, a.z), max(b.x, b.z)) == SENT) != 0; }
DEV void sb_arrive(lptr L, int k, int lane) { if (lane == 0) __hip_atomic_fetch_add((LAS unsigned*)(L + L_SYN) + k, 1u, __ATOMIC_RELAXED, __HIP_MEMORY_SCOPE_WORKGROUP); }
DEV void sb_wait(lptr L, int k, unsigned n) {
  for (unsigned spins = 0; spins < (SPIN_CAP << 4); ++spins) {
    if (__builtin_amdgcn_readfirstlane(__hip_atomic_load((LAS unsigned*)(L + L_SYN) + k, __ATOMIC_RELAXED, __HIP_MEMORY_SCOPE_WORKGROUP)) >= n) break;
    __builtin_amdgcn_s_sleep(1);
  }
  asm volatile("" ::: "memory");
}
DEV void wait_until(u64 t) { while ((long long)(__builtin_amdgcn_s_memrealtime() - t) < 0) __builtin_amdgcn_s_sleep(1); }
DEV void poll(const unsigned char* ws, int off, bool care, u64 tag) {
  for (unsigned spins = 0; spins < SPIN_CAP; ++spins) if (__all(!care || __hip_atomic_load((const GAS u64*)(ws + off), RLX_AGENT) == tag)) break;
  __builtin_amdgcn_fence(__ATOMIC_ACQUIRE, "wavefront");
}
DEV void set_flag(unsigned char* ws, int line, int idx, u64 tag) { __hip_atomic_store((GAS u64*)(ws + WS_FLAGS + line * 128) + idx, tag, RLX_AGENT); }
DEV int unit_row(int s, int i) {
  return s < 16 ? 36 * s + (i < 16 ? i : i + 4) : 36 * (8 * (s - 16) + 2 * ((i >> 2) & 3) + (i >> 4)) + 16 + (i & 3);
}

template <bool SAFE> DEV bool phi_maps(const f32x4 (&pv)[2][2], int gq, v2u (&pl)[2], v2u (&ml)[2], v2u (&hq)[2]) {
  f32x4 ep[2], em[2]; float hp[2][2], hm[2][2], sp[2], sm[2];
#pragma unroll
  for (int h = 0; h < 2; ++h) {
    const f32x4 lo = pv[h][0], hi = pv[h][1];
    float mx, mn;
    if (SAFE) {
      mx = red_max(fmaxf(fmaxf(fmaxf(lo.x, lo.y), fmaxf(lo.z, lo.w)), fmaxf(hi.x, hi.y))) * LOG2E;
      mn = red_min(fminf(fminf(fminf(lo.x, lo.y), fminf(lo.z, lo.w)), fminf(hi.x, hi.y))) * LOG2E;
    } else mx = mn = hi.x * LOG2E;
#pragma unroll
    for (int r = 0; r < 4; ++r) { ep[h][r] = __builtin_amdgcn_exp2f(__builtin_fmaf(lo[r], LOG2E, -mx)); em[h][r] = __builtin_amdgcn_exp2f(-__builtin_fmaf(lo[r], LOG2E, -mn)); }
    hp[h][0] = SAFE ? __builtin_amdgcn_exp2f(__builtin_fmaf(hi.x, LOG2E, -mx)) : 1.0f;
    hm[h][0] = SAFE ? __builtin_amdgcn_exp2f(-__builtin_fmaf(hi.x, LOG2E, -mn)) : 1.0f;
    hp[h][1] = __builtin_amdgcn_exp2f(__builtin_fmaf(hi.y, LOG2E, -mx)); hm[h][1] = __builtin_amdgcn_exp2f(-__builtin_fmaf(hi.y, LOG2E, -mn));
    sp[h] = (ep[h].x + ep[h].y) + (ep[h].z + ep[h].w); sm[h] = (em[h].x + em[h].y) + (em[h].z + em[h].w);
  }
  auto a0 = __builtin_amdgcn_permlane16_swap(fu(sp[0]), fu(sm[0]), false, false);
  auto a1 = __builtin_amdgcn_permlane16_swap(fu(sp[1]), fu(sm[1]), false, false);
  auto a2 = __builtin_amdgcn_permlane32_swap(fu(uf(a0[0]) + uf(a0[1])), fu(uf(a1[0]) + uf(a1[1])), false, false);
  const float h0 = (gq & 1) ? hm[0][0] + hm[0][1] : hp[0][0] + hp[0][1], h1 = (gq & 1) ? hm[1][0] + hm[1][1] : hp[1][0] + hp[1][1];
  const float tot = (uf(a2[0]) + uf(a2[1])) + (gq < 2 ? h0 : h1);
  const float rc = __builtin_amdgcn_rcpf(tot);
  auto b0 = __builtin_amdgcn_permlane32_swap(fu(rc), fu(rc), false, false);
#pragma unroll
  for (int h = 0; h < 2; ++h) {
    auto b1 = __builtin_amdgcn_permlane16_swap(b0[h], b0[h], false, false);
    const float rp = uf(b1[0]), rm = uf(b1[1]);
    pl[h] = pk4(ep[h] * rp); ml[h] = pk4(em[h] * rm);
    hq[h] = v2u{pk2(hp[h][0] * rp, hp[h][1] * rp), pk2(hm[h][0] * rm, hm[h][1] * rm)};
  }
  return tot < BIG;
}
DEV void phi_pair(const f32x4 (&pv)[2][2], int gq, v2u (&pl)[2], v2u (&ml)[2], v2u (&hq)[2]) {
  if (__builtin_expect(!__all(phi_maps<false>(pv, gq, pl, ml, hq)), 0)) phi_maps<true>(pv, gq, pl, ml, hq);
}
constexpr int fr_s(int i) { return i >= 90 ? (i - 90) / 2 : i / 5; }
constexpr int fr_t(int i) { return i >= 90 ? 5 + (i - 90) % 2 : i % 5; }
__host__ __device__ constexpr int fr_slot(int i) { return i < 35 ? 55 + i : i < 90 ? i - 35 : i; }
constexpr int xn(int u, int n) { const int up = n == 20 ? UPO : UPX, v = (up + 2 > 10 ? up + 2 : 10) + 2 * u; return u < 0 ? up : v < n ? v : n; }
template <int lo, class F, int... k> DEV void steps_(F&& f, std::integer_sequence<int, k...>) { (f(IC(lo + k)), ...); }
template <int lo, int hi, class F> DEV void steps(F&& f) { steps_<lo>(f, std::make_integer_sequence<int, hi - lo>{}); }

__global__ void __launch_bounds__(512, 2) fused_kernel(const u16* __restrict__ x0, const u16* __restrict__ wA, const u16* __restrict__ wphi, const u16* __restrict__ woA,
                                                       const u16* __restrict__ wB, const u16* __restrict__ woB, u16* __restrict__ out, unsigned char* ws0) {
  unsigned char* const ws = ws0;
  extern __shared__ __attribute__((aligned(16))) unsigned char smem[];
  const lptr L = (lptr)smem;
  const unsigned Lb = (unsigned)(uintptr_t)L;
  const int b = blockIdx.x, tid = threadIdx.x, lane0 = tid & 63, w0 = __builtin_amdgcn_readfirstlane(tid >> 6);
  const int p0 = ((b & 7) >> 1) * 4 + (b >> 6), g0 = (b & 1) * 8 + ((b >> 3) & 7);
  const rsrc_t rs = __builtin_amdgcn_make_buffer_rsrc((void*)ws, 0, WS_TOTAL, 0x00020000);
  v4u pf0, pf1;
  if (w0 == 7) {
    u64 pc; asm volatile("s_getpc_b64 %0" : "=s"(pc));
    const int ch = min(b >> 3, CODEPF - 1) * 2048 + lane0 * 16;
    asm volatile("global_load_dwordx4 %0, %2, %3\n\tglobal_load_dwordx4 %1, %2, %3 offset:1024" : "=&v"(pf0), "=&v"(pf1) : "v"(ch), "s"(pc & ~15ull) : "memory");
  }
  u64 tag;
  v4u xf[18];
  f32x4 res[3];
  v4u ring[15];
  float ssq;
  u64 t_go;
  const f32x4 z = {0.f, 0.f, 0.f, 0.f};
  auto colsums7 = [&](int type) {
    int lane = lane0;
    asm volatile("" : "+v"(lane));
    const int base = WS_CSP + (type * 16 + g0) * 18 * 448 + (lane >> 5) * 9 * 448 + (lane & 31) * 16;
    v4u c[9];
#pragma unroll
    for (int i = 0; i < 9; ++i) c[i] = gld128(rs, (lane & 31) < 28 ? base + i * 448 : OOB);
    f32x4 t = __builtin_bit_cast(f32x4, c[0]);
#pragma unroll
    for (int i = 1; i < 9; ++i) t += __builtin_bit_cast(f32x4, c[i]);
#pragma unroll
    for (int r = 0; r < 4; ++r) { auto sw = __builtin_amdgcn_permlane32_swap(fu(t[r]), fu(t[r]), false, false); t[r] = uf(sw[0]) + uf(sw[1]); }
    if (lane < 28) *(LAS f32x4*)(L + L_CS + type * 448 + lane * 16) = t;
  };
  auto wq_off = [&](int s, int r) {
    const int pc = r == 0 ? tid : (tid + 512 > 863 ? 863 : tid + 512), i = pc / 27, q = pc - i * 27;
    return unit_row(s, i) * 3456 + (q / 9) * 1080 + q * 8 + g0 * 72;
  };
  auto wq_put = [&](int img, v2u d0, v2u d1) { sts64(L, img + tid * 8, d0); if (tid + 512 < 864) sts64(L, img + (tid + 512) * 8, d1); };
  auto wq_emit = [&](int img, int type, int s, bool on = true) {
    const int w = w0, lane = lane0, tok = lane & 15, gq = lane >> 4, trq = (lane >> 2) & 3, trp = lane & 3;
    on = on && w < 7;
    {
      const int wco = (w & 1) * 40 + (w < 2 ? 72 : w < 4 ? 144 : 0) - (w > 4 ? 40 : 0) + (w == 6 ? 80 : 0);
      int a0 = img + (4 * gq + trq) * 216 + (w == 4 ? trp * 72 + 32 : wco + trp * 8), a1 = a0 + 16 * 216;
      if (w == 4 && trp == 3) a0 = a1 = L_ONEQ;
      const int u = s < 16 ? s + 2 : s - 16, j = w >= 5 ? 90 + 2 * u + w - 5 : 5 * u + w;
      const v4u f = cat(ldstr(L, a0), ldstr(L, a1));
      unsigned one2 = 0x3F803F80u; asm volatile("" : "+v"(one2));
      gst128(rs, on ? WS_WQ + (type * 16 + g0) * WQ_BLOB + fr_slot(j) * 1024 + lane * 16 : OOB, f);
      const f32x4 cs = mfma(f, v4u{one2, one2, one2, one2}, z);
      gst128(rs, on && tok == 0 ? WS_CSP + ((type * 16 + g0) * 18 + s) * 448 + w * 64 + gq * 16 : OOB, __builtin_bit_cast(v4u, cs));
    }
  };
  auto wo_off = [&](int s) { const int po = tid > 287 ? 287 : tid, io = po / 9; return unit_row(s, io) * 1152 + g0 * 72 + (po - io * 9) * 8; };
  auto wo_emit = [&](int img, int type, int s, int t, bool on) {
    const int lane = lane0, tok = lane & 15, gq = lane >> 4, trq = (lane >> 2) & 3, trp = lane & 3, g = g0;
    const int a0 = img + (4 * gq + trq) * 72 + (t == 2 ? 32 : t * 40 + trp * 8);
    const int o = WS_WO + (type * 16 + g) * WO_BLOB + 512 + (s < 16 ? 15 - s : 33 - s) * 2304 + (t < 2 ? t * 1024 + lane * 16 : 2048 + (gq * 4 + tok) * 16);
    gst128(rs, (!on || (t == 2 && tok >= 4)) ? OOB : o, cat(ldstr(L, a0), ldstr(L, a0 + 16 * 72)));
  };
  auto st_kvp = [&](int i2, int k, v2u v) {
    int lane = lane0, w = w0;
    asm volatile("" : "+v"(lane), "+s"(w));
    const int tok = lane & 15, gq = lane >> 4, kh = w >= 3 ? 1 : 0, kft = w - 3 * kh;
    const int rb = WS_KVP + ((i2 * 16 + g0) * 16 + p0) * KVP_REC + kh * 1536, slot = (tok >> 1) == 2 ? 2 + (tok & 1) : (tok >> 1) == kh ? (tok & 1) : -1;
    const int o = kft < 2 ? (k == 0 ? lane * 16 : 1024 + (gq * 4 + slot) * 16) + kft * 8 : (k == 0 ? 1280 + tok * 8 : 1408 + slot * 8);
    if (w < 6 && (kft < 2 || gq == 0) && (k == 0 || slot >= 0)) gst64(rs, rb + o, v);
  };
  auto mark_kvp = [&](int i2) { unsigned sv = SENT; asm volatile("" : "+v"(sv)); st_kvp(i2, 0, v2u{sv, sv}); st_kvp(i2, 1, v2u{sv, sv}); };
  {
    const int lane = lane0, tok = lane & 15, gq = lane >> 4, w = w0, p = p0, g = g0, T = 8 * p + w;
    constexpr int RAW_A = 147456;
    v2u dA[2], dE[2], dB[2], dO[3], r0, r1, r2, ep, dP;
    aload64p(dA[0], wA, wq_off(p, 0)); if (w < 6) aload64p(dA[1], wA, wq_off(p, 1));
    aload64(ep, ws, WS_EPOCH);
    if (p < 2) { aload64p(dE[0], wA, wq_off(16 + p, 0)); if (w < 6) aload64p(dE[1], wA, wq_off(16 + p, 1)); }
    PIN();
    auto x_dma = [&](auto i_) {
      constexpr int i = decltype(i_)::value;
      const int ci = i * 64 + lane, r = (ci * 911) >> 16;
      const int src = T * 18432 + ci * 16 - r * 16 + (ci - r * 72 < r ? 1152 : 0);
      dma_in(x0, src, Lb + w * 18432 + i * 1024);
    };
    steps<0, XB1>(x_dma);
    if (tid == 0) { sts128(L, L_ONEQ, v4u{0x3F80u, 0u, 0u, 0u}); sts128(L, L_SYN, v4u{0u, 0u, 0u, 0u}); }
    VMWAIT(XB1); PIN();
    asm volatile("" : "+v"(dA[0]), "+v"(dA[1]), "+v"(dE[0]), "+v"(dE[1]), "+v"(ep));
    asm volatile("" :: "v"(pf0), "v"(pf1));
    wq_put(RAW_A, dA[0], dA[1]);
    tag = ((u64)(unsigned)__builtin_amdgcn_readfirstlane(ep.x) | ((u64)(unsigned)__builtin_amdgcn_readfirstlane(ep.y) << 32)) + 1;
    BAR();
    wq_emit(RAW_A, 0, p);
    mark_kvp(0);
    if (p < 2) { BAR(); wq_put(RAW_A, dE[0], dE[1]); BAR(); wq_emit(RAW_A, 0, 16 + p); }
    PIN();
    steps<XB1, XB2>(x_dma);
    VMWAIT(XB2 - XB1);
    RAWBAR();
    if (tid == 448) set_flag(ws, FL_RT1 + g, p, tag);
    steps<XB2, 18>(x_dma);
    const int xr = (16 * T + tok) * 1152 + 72 * g;
    const int phi_i = tid < 162 ? tid : 161;
    if (w < 3) aload64p(dP, wphi, g * 1296 + phi_i * 8);
    PIN();
    auto later = [&]() {
      aload64p(r0, x0, xr + 8 * gq); aload64p(r1, x0, xr + 40 + 8 * gq); aload64p(r2, x0, xr + 32);
      aload64p(dB[0], wB, wq_off(p, 0)); if (w < 6) aload64p(dB[1], wB, wq_off(p, 1));
      if (w < 5) { aload64p(dO[0], woA, wo_off(p)); aload64p(dO[1], woB, wo_off(p)); }
      if (p >= 2 && p < 4) { aload64p(dE[0], wB, wq_off(16 + (p & 1), 0)); if (w < 6) aload64p(dE[1], wB, wq_off(16 + (p & 1), 1)); }
      else if (p >= 4 && p < 8 && w < 5) aload64p(dO[2], p < 6 ? woA : woB, wo_off(16 + (p & 1)));
    };
    int xo[36];
    {
      const int xb = w * 18432 + tok * 1152;
      auto xoff = [&](int byte) { int c = (byte >> 4) + tok; c -= c >= 72 ? 72 : 0; return xb + c * 16 + (byte & 8); };
#pragma unroll
      for (int s = 0; s < 16; ++s) { xo[2 * s] = xoff(72 * s + 8 * gq); xo[2 * s + 1] = xoff(72 * s + 40 + 8 * gq); }
#pragma unroll
      for (int s = 0; s < 4; ++s) xo[32 + s] = xoff(72 * (8 * (s >> 1) + 2 * gq + (s & 1)) + 32);
#pragma unroll
      for (int i = 0; i < 36; ++i) asm volatile("" : "+v"(xo[i]));
    }
    DRAIN();
#pragma unroll
    for (int s = 0; s < 18; ++s) xf[s < 16 ? s + 2 : s - 16] = cat(lds64(L, xo[2 * s]), lds64(L, xo[2 * s + 1]));
    v2u pv;
    if (w == 0) aload64(pv, ws, poll_off(FL_RT1 + g, FL_RT1 + g, lane));
    later();
    PIN();
    {
      float q4[4] = {0.f, 0.f, 0.f, 0.f};
#pragma unroll
      for (int s = 0; s < 18; ++s)
#pragma unroll
        for (int j = 0; j < 4; ++j) q4[j] = dot2(xf[s][j], q4[j]);
      ssq = (q4[0] + q4[1]) + (q4[2] + q4[3]);
    }
    PIN();
    if (w == 0) {
      asm volatile("s_waitcnt vmcnt(0)" : "+v"(pv) :: "memory");
      if (!__all(((u64)pv.y << 32 | pv.x) == tag)) poll(ws, poll_off(FL_RT1 + g, FL_RT1 + g, lane), true, tag);
    }
    BAR();
    if (w == 7) colsums7(0);
#pragma unroll 1
    for (int k = 0; k < 8; ++k) dmaf(ws, WS_WQ + g * WQ_BLOB, Lb, fr_slot(k * 8 + w), lane * 16);
    VMWAIT(8); PIN();
    asm volatile("" : "+v"(r0), "+v"(r1), "+v"(r2), "+v"(dP), "+v"(dB[0]), "+v"(dB[1]), "+v"(dE[0]), "+v"(dE[1]), "+v"(dO[0]), "+v"(dO[1]), "+v"(dO[2]));
    wq_put(RAW_B, dB[0], dB[1]);
    if (p < 4) wq_put(RAW_E1, dE[0], dE[1]);
    if (tid < 288) { sts64(L, RAW_OA + tid * 8, dO[0]); sts64(L, RAW_OB + tid * 8, dO[1]); if (p >= 4) sts64(L, RAW_E1 + tid * 8, dO[2]); }
    if (tid < 162) { const int d0 = 2 * phi_i, d1 = d0 + 1; *(LAS unsigned*)(L + L_PH + (d0 / 9) * 4 + d0 * 4) = dP.x; *(LAS unsigned*)(L + L_PH + (d1 / 9) * 4 + d1 * 4) = dP.y; }
    if (tid >= 448 && tid < 448 + 46) { const int i = tid - 448; *(LAS unsigned*)(L + L_PH + (i < 36 ? i * 40 + 36 : 1440 + (i - 36) * 4)) = 0u; }
    res[0] = unpk4(r0); res[1] = unpk4(r1); res[2] = unpk4(r2);
    PIN();
    VMWAIT(3);
    BAR();
#pragma unroll
    for (int i = 0; i < RING; ++i) ring[i] = lds128(L, fr_slot(i) * 1024 + lane * 16);
    PIN();
  }

  auto layer = [&](auto ty_, auto first_, const int it) __attribute__((always_inline)) {
    constexpr int ty = decltype(ty_)::value;
    constexpr bool first = decltype(first_)::value;
    const int Lr = 2 * it + ty;
    int lane = lane0, w = w0, p = p0, g = g0;
    unsigned char* ws = ws0;
    asm volatile("" : "+v"(lane), "+s"(w), "+s"(p), "+s"(g), "+s"(ws));
    const int T = 8 * p + w;
    const int tok = lane & 15, gq = lane >> 4, trq = (lane >> 2) & 3, trp = lane & 3, l16 = lane * 16;
    const bool last = (ty == 1) && (it == 1);
    int hi16 = l16 + 65536;
    asm volatile("" : "+v"(hi16));
    auto wfrag = [&](int slot) { return slot < 64 ? lds128(L, l16 + slot * 1024) : lds128(L, hi16 + (slot - 64) * 1024); };
    const int curW = WS_WQ + (ty * 16 + g) * WQ_BLOB, nxtW = WS_WQ + ((1 - ty) * 16 + g) * WQ_BLOB, curO = WS_WO + (ty * 16 + g) * WO_BLOB;
    const int flm = FL_HOP + Lr * 16;
    v4u rv[4];
    const bool old = w < 4;
    auto mark = [&](int k, int base, bool on) {
      unsigned sv = SENT; asm volatile("" : "+v"(sv));
      gst128(rs, !on ? OOB : k == 0 ? base + l16 : gq == 0 ? base + tok * 16 : OOB, v4u{sv, sv, sv, sv});
    };
    auto mark_x = [&](int k, int Lx) { mark(k, WS_FX + Lx * FRAG_BUF + T * FT + (k ? 16384 + g * 256 : g * 1024), Lx < 3); };
    auto mark_o = [&](int k, int Lo) { mark(k, WS_FO + Lo * FRAG_BUF + T * FT + (k ? 16384 + g * 256 : g * 1024), Lo < 4); };
    auto mark_kv = [&](int k) { mark(k == 2, WS_KVX + it * KVX_BUF + (T * 16 + g) * KVX_REC + k * 1024, true); };
    auto got2 = [&](auto n_, v4u& a, v4u& b, auto&& again) {
      asm volatile("s_waitcnt vmcnt(%2)" : "+v"(a), "+v"(b) : "n"(decltype(n_)::value) : "memory");
      if (__builtin_expect(unwritten(a, b), 0)) {
#pragma unroll 1
        for (unsigned spins = 0; spins < SPIN_CAP; ++spins) {
          __builtin_amdgcn_s_sleep(RETRY_SLEEP);
          again();
          asm volatile("s_waitcnt vmcnt(0)" : "+v"(a), "+v"(b) :: "memory");
          if (!unwritten(a, b)) break;
        }
      }
    };
    int pv0, pvr;
    auto pl_at = [&](int base) { pv0 = base + T * FT + 4096 + l16; pvr = base + T * FT + 16384 + gq * 512 + tok * 16; };
    auto pl_op = [&](auto k_) {
      constexpr int k = decltype(k_)::value;
      if constexpr (k < 4) aload<(k & 1) * 256 + (k >> 1) * 2048>(rv[k], ws, pvr);
      else if constexpr (k < 20) aload<((k - 4) % 8) * 1024 - 4096>(xf[k - 2], ws, k < 12 ? pv0 : pv0 + 8192);
      else if constexpr (k < 22) dmaf(ws, curW, Lb, min(Q_LATE + (k - 20) * 8 + w, 125), l16);
      else if constexpr (k < 26) dmaf(ws, curO, Lb + L_O, min(O_LATE + (k - 22) * 8 + w, 40), l16);
      else mark_kv(k - 26);
    };
    auto pl_step = [&](auto s_, auto n_) {
      constexpr int s = decltype(s_)::value, n = decltype(n_)::value;
      PIN();
      if (s == 0) {
        got2(IC(xn(-1, n) - 2), rv[0], rv[1], [&] { steps<0, 2>(pl_op); });
        got2(IC(xn(-1, n) - 4), rv[2], rv[3], [&] { steps<2, 4>(pl_op); });
        xf[0] = v4u{rv[0].x, rv[0].y, rv[1].x, rv[1].y}; xf[1] = v4u{rv[2].x, rv[2].y, rv[3].x, rv[3].y};
        PIN();
      }
      steps<xn(s - 1, n), xn(s, n)>(pl_op);
      PIN();
      if constexpr (s >= 2 && s % 2 == 0) got2(IC(xn(s, n) - s - 4), xf[s], xf[s + 1], [&] { steps<s + 2, s + 4>(pl_op); });
      PIN();
    };
    if (!first) {
      pl_at(WS_FX + (Lr - 1) * FRAG_BUF);
      wait_until(t_go);
      steps<0, UPX>(pl_op);
    }
    PIN();
    f32x4 acc[7];
#pragma unroll
    for (int t = 0; t < 7; ++t) acc[t] = z;
    const int moff = first && lane >= 32 ? poll_off(FL_CP + p, FL_CP + p, lane) : poll_off(flm + g, flm + g, lane);
    auto kv_fetch = [&]() {
#pragma unroll
      for (int j = 0; j < 2; ++j) {
        const int Tg = 8 * (p - 2) + w + 8 * j, ib = (w + 8 * j) * IMG_REC;
        if (Tg >= 0) {
          const int rb = WS_KVX + it * KVX_BUF + (Tg * 16 + g) * KVX_REC;
          dmaf(ws, rb, Lb + ib, 0, l16); dmaf(ws, rb, Lb + ib, 1, l16);
          if (lane < 16) dmaf(ws, rb, Lb + ib, 2, l16);
        } else {
          unsigned zz = 0u; asm volatile("" : "+v"(zz));
          sts128(L, ib + l16, v4u{zz, zz, zz, zz}); sts128(L, ib + 1024 + l16, v4u{zz, zz, zz, zz});
          if (lane < 16) sts128(L, ib + 2048 + l16, v4u{zz, zz, zz, zz});
        }
      }
    };
    auto qkv_step = [&](auto i_) {
      constexpr int i = decltype(i_)::value, s = fr_s(i), t = fr_t(i);
      if (i < 90 && t == 0) {
        PIN();
        if (!first) {
          pl_step(IC(s), IC(ty == 0 ? 29 : 26));
          if (s == 0) { if (old) sb_arrive(L, 1, lane); }
          if (s == 0) ssq = (uf(rv[0].z) + uf(rv[1].z)) + (uf(rv[2].z) + uf(rv[3].z));
        } else {
          if (s < 4) { dmaf(ws, curW, Lb, fr_slot((8 + 2 * s) * 8 + w), l16); dmaf(ws, curW, Lb, fr_slot(min((9 + 2 * s) * 8 + w, 125)), l16); }
          if (s == 5) { VMWAIT(8 + 2); RAWBAR(); }
          if (s == 4) wq_emit(RAW_B, 1, p);
          if (s == 5) wq_emit(RAW_E1, 1, 16 + (p & 1), p >= 2 && p < 4);
          if (s == 6) wo_emit(w < 3 ? RAW_OA : RAW_OB, w < 3 ? 0 : 1, p, w % 3, w < 6);
          if (s == 7) wo_emit(RAW_E1, p < 6 ? 0 : 1, 16 + (p & 1), w % 3, p >= 4 && p < 8 && w < 3);
          if (s == 10) { VMWAIT(6); RAWBAR(); }
          if (s >= 10 && s < 12) mark_o(s - 10, 0);
          if (s >= 12 && s < 15) mark_kv(s - 12);
          if (s == 15) mark_kvp(1);
        }
        PIN();
      }
      if (i == 19 && !first) { PIN(); sb_wait(L, 1, 8 * Lr); PIN(); }
      if (i == 80 && ty == 0) RAWBAR();
      if (i == 108 || i == 112) { mark_x(i == 112, Lr); PIN(); }
      if (ty == 0 && !first && i >= 106 && i < 126 && i % 4 == 2) { dmaf(ws, nxtW, Lb, min(55 + (i - 106) * 2 + w, 89), l16); PIN(); }
      if (i == FI && ty == 1) { PIN(); wait_until(t_go); kv_fetch(); PIN(); }
      acc[t] = mfma(ring[i % RING], xf[s], acc[t]);
      if (i + RING < 126) ring[i % RING] = wfrag(fr_slot(i + RING));
      PINM();
    };
    const int csb = L_CS + ty * 448 + gq * 16;
    auto wph = [&](int h, int ft) {
      const int rl = L_PH + (20 * h + 4 * gq + trq) * 40, ru = L_PH + ((trq >> 1) == h ? (18 * h + (h ? 0 : 16) + (trq & 1)) * 40 : 1440);
      return cat(ldstr(L, rl + (ft ? 32 : trp * 8)), ldstr(L, ru + (ft ? 32 : trp * 8)));
    };
    const int vsw = gq * 128 + ((tok * 8) ^ ((gq >> 1) * 64));
    const int trl = trp * 128 + (((4 * gq + trq) * 8) ^ ((trp >> 1) * 64));
    v4u wp[2][2]; f32x4 cs[5];
    steps<0, 78>(qkv_step);
#pragma unroll
    for (int t = 0; t < 5; ++t) cs[t] = *(LAS f32x4*)(L + csb + t * 64);
    if (ty == 0) {
#pragma unroll
      for (int h = 0; h < 2; ++h) { wp[h][0] = wph(h, 0); wp[h][1] = wph(h, 1); }
    }
    PIN();
    steps<78, 90>(qkv_step);
    const float mean = hi_half(odd_rows(acc[4].x)) * (1.0f / 576.0f);
    const float var = red_add(ssq) * (1.0f / 576.0f) - mean * mean;
    const float rstd = __builtin_amdgcn_rsqf(fmaxf(var, 0.f) + 1e-5f);
    auto fix = [&](int t, float sc) { return (acc[t] - mean * (t < 5 ? cs[t < 5 ? t : 0] : *(LAS f32x4*)(L + csb + t * 64))) * sc; };
    v2u kq[2], vq[2];
    kq[0] = pk4(fix(0, rstd)); kq[1] = pk4(fix(1, rstd));
    const f32x4 remf = fix(4, rstd);
    const v2u remq = pk4(remf);
    const int vpos = gq * 256 + ((tok * 16) ^ ((gq >> 1) * 128));
    v4u kboth, vboth;
    const int kh = w >= 3 ? 1 : 0, kft = w - 3 * kh;
    if (ty == 0) {
      const v2u ku = gq == 1 ? remq : v2u{0u, 0u};
      f32x4 pv[2][2];
#pragma unroll
      for (int h = 0; h < 2; ++h) { const v4u bk = cat(kq[h], ku); pv[h][0] = mfma(wp[h][0], bk, z); pv[h][1] = mfma(wp[h][1], bk, z); }
      const int sb = L_STG + w * STG_REC;
      vq[0] = pk4(fix(2, rstd)); vq[1] = pk4(fix(3, rstd));
      sts64(L, sb + vsw, vq[0]); sts64(L, sb + 512 + vsw, vq[1]);
      if (gq == 2) sts64(L, sb + 3072 + tok * 8, remq);
      if (lane >= 62) sts64(L, sb + 3456 + (lane & 1) * 8, v2u{(lane & 1) ? 0u : 0x3F80u, 0u});
      PIN();
      v2u pl[2], ml[2], hq[2];
      phi_pair(pv, gq, pl, ml, hq);
#pragma unroll
      for (int h = 0; h < 2; ++h) {
        sts64(L, sb + 1024 + h * 1024 + vsw, pl[h]); sts64(L, sb + 1536 + h * 1024 + vsw, ml[h]);
        if (gq == 0) sts64(L, sb + 3200 + h * 128 + tok * 8, hq[h]);
      }
      if (!first) DRAIN();
      BAR();
      if (w < 6) {
        const int aa = L_STG + (kft < 2 ? 1024 + kh * 1024 + kft * 512 + trl : (trp == 0 ? 3200 + kh * 128 + (4 * gq + trq) * 8 : 3464));
        const int bl = L_STG + kh * 512 + trl, bh = L_STG + (trp == 0 ? 3072 + (4 * gq + trq) * 8 : 3456);
        v4u fa[4], fl[4], fh[4];
#pragma unroll
        for (int ks = 0; ks < 4; ++ks) {
          const int o0 = 2 * ks * STG_REC, o1 = o0 + STG_REC;
          fa[ks] = cat(ldstr(L, aa + o0), ldstr(L, aa + o1)); fl[ks] = cat(ldstr(L, bl + o0), ldstr(L, bl + o1)); fh[ks] = cat(ldstr(L, bh + o0), ldstr(L, bh + o1));
        }
        PIN();
        f32x4 dl = z, dh = z;
#pragma unroll
        for (int ks = 0; ks < 4; ++ks) { dl = mfma(fa[ks], fl[ks], dl); dh = mfma(fa[ks], fh[ks], dh); }
        st_kvp(it, 0, pk4(dl)); st_kvp(it, 1, pk4(dh));
      }
      t_go = __builtin_amdgcn_s_memrealtime() + HOP_P;
      if (w >= 6) {
#pragma unroll
        for (int j = 0; j < 4; ++j) { unsigned zz = 0u; asm volatile("" : "+v"(zz)); sts128(L, (w - 6) * 4096 + j * 1024 + l16, v4u{zz, zz, zz, zz}); }
      }
    } else {
      const int rb = WS_KVX + it * KVX_BUF + (T * 16 + g) * KVX_REC;
      gst128(rs, rb + l16, kboth = (gq & 1) ? cat(kq[1], kq[0]) : cat(kq[0], kq[1]));
      if (gq == 1 || gq == 2) gst64(rs, rb + 2048 + (gq - 1) * 128 + tok * 8, remq);
      vq[0] = pk4(fix(2, rstd)); vq[1] = pk4(fix(3, rstd));
      gst128(rs, rb + 1024 + vpos, vboth = (gq & 1) ? cat(vq[1], vq[0]) : cat(vq[0], vq[1]));
      VMWAIT(3);
      RAWBAR();
      t_go = __builtin_amdgcn_s_memrealtime() + HOP_M;
      const int ib = (16 + w) * IMG_REC;
      sts128(L, ib + l16, kboth); sts128(L, ib + 1024 + vpos, vboth);
      if (gq == 1 || gq == 2) sts64(L, ib + 2048 + (gq - 1) * 128 + tok * 8, remq);
      if (lane < 3) { const int i = w + 8 * lane; sts128(L, i * IMG_REC + 2304, v4u{0x3F80u, 0u, (8 * (p - 2) + i >= 0) ? 0u : 0xF149u, 0u}); }
    }
    PIN();
    if (first) {
      steps<90, QP>(qkv_step);
      DRAIN();
      RAWBAR();
      if (tid == 448) { set_flag(ws, flm + g, p, tag); set_flag(ws, FL_CP + p, g, tag); }
    }
    PIN();
    steps<first ? QP : 90, 126>(qkv_step);
    if (ty == 0) sb_arrive(L, 2, lane);
    v4u dp[16];
    const int pi = w * 64 + lane;
    constexpr float QS = 0.23570226039551584f * LOG2E;
    const v2u q0 = pk4(fix(5, ty == 1 ? rstd * QS : rstd)), q1 = pk4(fix(6, ty == 1 ? rstd * QS : rstd));
    v2u olo[2]; unsigned ohi[2];
    if (ty == 0) {
      const int h = pi >= 96 ? 1 : 0, j = pi - 96 * h;
      const int po = j < 90 ? WS_KVP + (it * 16 + g) * 16 * KVP_REC + pi * 16 : OOB;
      if (w < 3) {
        wait_until(t_go);
#pragma unroll
        for (int q = 0; q < 16; ++q) aloadb(dp[q], rs, po, q * KVP_REC);
      }
      PIN();
      v4u qf[2][2];
      {
        const v2u qu = gq == 0 ? remq : v2u{0u, 0u};
        f32x4 pv[2][2];
#pragma unroll
        for (int h = 0; h < 2; ++h) { const v4u bq = cat(h ? q1 : q0, qu); pv[h][0] = mfma(wp[h][0], bq, z); pv[h][1] = mfma(wp[h][1], bq, z); }
        v2u pl[2], ml[2], hq[2];
        phi_pair(pv, gq, pl, ml, hq);
#pragma unroll
        for (int h = 0; h < 2; ++h) { qf[h][0] = cat(pl[h], ml[h]); qf[h][1] = cat(hq[h], v2u{0u, 0u}); }
      }
      PIN();
      if (w >= 6) {
        if (first) { poll(ws, moff, true, tag); if (w == 7) colsums7(1); }
        else __builtin_amdgcn_s_sleep(SLP67);
        DRAIN();
        sb_wait(L, 2, 8 * (it + 1));
#pragma unroll 1
        for (int f = w - 6; f < (first ? 41 : O_LATE); f += 2) dmaf(ws, curO, Lb + L_O, f, l16);
        if (!first) {
#pragma unroll 1
          for (int f = 90 + w - 6; f < Q_LATE; f += 2) dmaf(ws, nxtW, Lb, f, l16);
        }
      } else if (w >= 3) DRAIN();
      else {
        DRAIN(); PIN();
#pragma unroll
        for (int q = 0; q < 16; ++q) asm volatile("" : "+v"(dp[q]));
#pragma unroll 1
        for (unsigned spins = 0; spins < SPIN_CAP; ++spins) {
          unsigned m = 0u;
#pragma unroll
          for (int q = 0; q < 16; ++q) m = max(max(dp[q].x, dp[q].z), m);
          if (__builtin_amdgcn_ballot_w64(m == SENT) == 0) break;
#pragma unroll
          for (int q = 0; q < 16; ++q) if (unwritten(dp[q], dp[q])) dp[q] = gld128(rs, po, q * KVP_REC);
        }
        f32x4 sa = z, sb2 = z;
        unsigned c10 = 0x00003F80u, c01 = 0x3F800000u; asm volatile("" : "+v"(c10), "+v"(c01));
        auto acc2 = [&](unsigned v, unsigned c, float a) { return __builtin_amdgcn_fdot2_f32_bf16(__builtin_bit_cast(bf16x2, v), __builtin_bit_cast(bf16x2, c), a, false); };
#pragma unroll
        for (int q = 0; q < 16; ++q) {
          sa.x = acc2(dp[q].x, c10, sa.x); sa.y = acc2(dp[q].x, c01, sa.y); sa.z = acc2(dp[q].y, c10, sa.z); sa.w = acc2(dp[q].y, c01, sa.w);
          sb2.x = acc2(dp[q].z, c10, sb2.x); sb2.y = acc2(dp[q].z, c01, sb2.y); sb2.z = acc2(dp[q].w, c10, sb2.z); sb2.w = acc2(dp[q].w, c01, sb2.w);
        }
        const v2u va = pk4(sa), vb = pk4(sb2);
        const int e = j - 64, sl = e & 3;
        const bool isz = (j >= 64 && j < 80 && sl == 2) || j == 89;
        const int da = h * 4096 + (j < 64 ? j * 16 : j < 80 ? 2048 + ((e >> 2) * 16 + (sl < 2 ? 2 * h + sl : 4)) * 16 : j < 88 ? 1024 + (j - 80) * 32 : j == 88 ? 3072 + h * 32 : 3072 + 64);
        const int db = da + (j < 80 ? 8 : 16);
        sb_wait(L, 2, 8 * (it + 1));
        if (j < 90 && !(j >= 64 && j < 80 && sl == 3)) { sts64(L, da, va); if (j != 89) sts64(L, db, vb); }
        if (isz) { sts64(L, da + 128, va); if (j != 89) sts64(L, db + 128, vb); }
      }
      BAR();
      if (!first && tid == 448) set_flag(ws, flm + g, p, tag);
      v4u kf[2][4];
#pragma unroll
      for (int h = 0; h < 2; ++h)
#pragma unroll
        for (int j = 0; j < 4; ++j) kf[h][j] = lds128(L, h * 4096 + j * 1024 + l16);
      PIN();
#pragma unroll
      for (int h = 0; h < 2; ++h) {
        const f32x4 dl2 = mfma(kf[h][1], qf[h][1], mfma(kf[h][0], qf[h][0], z));
        const f32x4 dh2 = mfma(kf[h][3], qf[h][1], mfma(kf[h][2], qf[h][0], z));
        const float r = __builtin_amdgcn_rcpf(odd_rows(dh2.x) + 1e-6f);
        olo[h] = pk4(dl2 * r); ohi[h] = h == 0 ? pk2(dh2.x * r, dh2.y * r) : pk2(dh2.z * r, dh2.w * r);
      }
    } else {
      v4u qf[2];
      {
        const v2u qr = pk4(remf * QS);
        const unsigned bias1 = gq == 1 ? 0x3F80u : 0u;
        qf[0] = cat(q0, v2u{gq == 0 ? qr.x : bias1, 0u});
        qf[1] = cat(q1, v2u{bias1, gq == 0 ? qr.y : 0u});
      }
      const int wb = w * IMG_REC;
      const int kuo = wb + (gq == 1 ? 2312 : 2048 + tok * 8);
      auto kfrag = [&](int h, int i) { return cat(lds64(L, wb + ((h ^ (gq & 1)) * 8) + l16 + i * IMG_REC), lds64(L, kuo + i * IMG_REC)); };
      float nm[2] = {0.f, 0.f};
      const int vl = wb + 1024 + trp * 256 + (((4 * gq + trq) * 16) ^ ((trp >> 1) * 128));
      const int vh = wb + (trp == 0 ? 2176 + (4 * gq + trq) * 8 : 2304);
      const unsigned m0 = (4 * gq + 0 <= tok ? 0xffffu : 0u) | (4 * gq + 1 <= tok ? 0xffff0000u : 0u);
      const unsigned m1 = (4 * gq + 2 <= tok ? 0xffffu : 0u) | (4 * gq + 3 <= tok ? 0xffff0000u : 0u);
      PIN();
#pragma unroll 1
      for (unsigned spins = 0; spins < SPIN_CAP; ++spins) {
        DRAIN();
        const int i0 = wb + l16, i1 = i0 + 8 * IMG_REC;
        const v4u c0 = lds128(L, i0), c1 = lds128(L, i0 + 1024), c2 = lds128(L, i1), c3 = lds128(L, i1 + 1024), rr = lds128(L, wb + (gq & 1) * 8 * IMG_REC + 2048 + tok * 16);
        if (__builtin_expect(!(unwritten(c0, c1) | unwritten(c2, c3) | unwritten(rr, rr)), 1)) break;
        __builtin_amdgcn_s_sleep(RETRY_SLEEP);
        kv_fetch();
      }
      BAR();
#pragma unroll 1
      for (int pass = 0; pass < 2; ++pass) {
        v4u kf[34]; f32x4 sc[2][17]; v4u va, van, vb[9]; f32x4 dl, dh;
        bool ok = true;
        constexpr int ord[17] = {16, 0, 1, 2, 3, 4, 5, 6, 7, 8, 9, 10, 11, 12, 13, 14, 15};
#pragma unroll
        for (int j = 0; j < 8; ++j) kf[j] = kfrag(0, ord[j]);
        auto vread = [&](int h, int ks) {
          const int i0 = ks == 0 ? 0 : 2 * ks - 1, i1 = ks == 0 ? 16 : ks < 8 ? 2 * ks : 15, vo = vl + ((h ^ (trp & 1)) * 8);
          van = cat(ldstr(L, vo + i0 * IMG_REC), ldstr(L, vo + i1 * IMG_REC));
          if (h == 0) vb[ks] = cat(ldstr(L, vh + i0 * IMG_REC), ldstr(L, vh + i1 * IMG_REC));
        };
        auto qk = [&](int j) { const int h = j / 17; sc[h][ord[j % 17]] = mfma(kf[j], qf[h], f32x4{nm[h], nm[h], nm[h], nm[h]}); };
        auto pexp = [&](f32x4 x) {
          f32x4 e;
#pragma unroll
          for (int r = 0; r < 4; ++r) e[r] = __builtin_amdgcn_exp2f(x[r]);
          return pk4(e);
        };
        vread(0, 0);
        PIN();
#pragma unroll
        for (int j = 0; j < 4; ++j) qk(j);
        PIN();
        auto att_step = [&](auto n_) {
          constexpr int n = decltype(n_)::value, h = n / 9, ks = n % 9;
          va = van;
          if (n < 17) vread((n + 1) / 9, (n + 1) % 9);
          if constexpr (2 * n + 8 < 34) kf[2 * n + 8] = kfrag((2 * n + 8) / 17, ord[(2 * n + 8) % 17]);
          if constexpr (2 * n + 9 < 34) kf[2 * n + 9] = kfrag((2 * n + 9) / 17, ord[(2 * n + 9) % 17]);
          if (2 * n + 4 < 34) qk(2 * n + 4);
          if (2 * n + 5 < 34) qk(2 * n + 5);
          v4u bp;
          if constexpr (ks == 0) {
            f32x4 mg;
#pragma unroll
            for (int r = 0; r < 4; ++r) mg[r] = (4 * gq + r <= tok) ? sc[h][16][r] : sc[h][0][r];
            const v2u p0 = pexp(mg);
            bp = v4u{p0.x & ~m0, p0.y & ~m1, p0.x & m0, p0.y & m1};
          } else bp = cat(pexp(sc[h][2 * ks - 1]), ks < 8 ? pexp(sc[h][ks < 8 ? 2 * ks : 0]) : v2u{0u, 0u});
          dl = mfma(va, bp, ks == 0 ? z : dl); dh = mfma(vb[ks], bp, ks == 0 ? z : dh);
          int wo_ = w; asm volatile("" : "+s"(wo_));
          if (n < 2) dmaf(ws, curO, Lb + L_O, min(n * 8 + wo_, O_LATE - 1), l16);
          else if (n < 10 && !last) dmaf(ws, nxtW, Lb, min(55 + (n - 2) * 8 + wo_, Q_LATE - 1), l16);
          if (ks == 8) {
            const float l = odd_rows(dh.x);
            ok = ok && l < BIG && l > 1.0f / BIG;
            const float r = __builtin_amdgcn_rcpf(l);
            olo[h] = pk4(dl * r); ohi[h] = h == 0 ? pk2(dh.x * r, dh.y * r) : pk2(dh.z * r, dh.w * r);
          }
          PIN();
        };
        steps<0, 18>(att_step);
        if (__builtin_expect(__all(ok), 1)) break;
#pragma unroll
        for (int h = 0; h < 2; ++h) {
          const f32x4 s0 = mfma(kfrag(h, 0), qf[h], z), s16 = mfma(kfrag(h, 16), qf[h], z);
          float m = -3e38f;
#pragma unroll
          for (int r = 0; r < 4; ++r) m = fmaxf(m, (4 * gq + r <= tok) ? s16[r] : s0[r]);
#pragma unroll 1
          for (int i = 1; i < 16; ++i) { const f32x4 s = mfma(kfrag(h, i), qf[h], z); m = fmaxf(fmaxf(fmaxf(m, s.x), fmaxf(s.y, s.z)), s.w); }
          nm[h] = -red_max(m);
        }
      }
    }
    gst128(rs, WS_FO + Lr * FRAG_BUF + T * FT + g * 1024 + l16, cat(olo[0], olo[1]));
    gst128(rs, gq == 0 ? WS_FO + Lr * FRAG_BUF + T * FT + 16384 + g * 256 + tok * 16 : OOB, v4u{ohi[0], ohi[1], 0u, 0u});
    t_go = __builtin_amdgcn_s_memrealtime() + HOP_O;
    if (ty == 0 && !first && w == 7) poll(ws, moff, true, tag);
    VMWAIT(2);
    if (first) RAWBAR();
    else sb_arrive(L, 0, lane);
    if (first) dma_range<8>(ws, nxtW, Lb, 55, 90, w, l16);
    mark_o(0, Lr + 1); mark_o(1, Lr + 1);
    int orem = L_O + 512 + 2048 + (gq * 4 + (tok & 3)) * 16, ol16 = L_O + 512 + l16;
    asm volatile("" : "+v"(orem), "+v"(ol16));
    auto ofrag = [&](int i) { return lds128(L, (17 - i / 3) * 2304 + (i % 3 == 2 ? orem : (i % 3) * 1024 + ol16)); };
#pragma unroll
    for (int i = 0; i < 15; ++i) ring[i] = ofrag(i);
    PIN();
    pl_at(WS_FO + Lr * FRAG_BUF);
    wait_until(t_go);
    steps<0, UPO>(pl_op);
    steps<0, 54>([&](auto i_) {
      constexpr int i = decltype(i_)::value;
      if (i % 3 == 0) pl_step(IC(i / 3), IC(20));
      if (first && i == 0) sb_arrive(L, 0, lane);
      if (i == 21) { PIN(); sb_wait(L, 0, 8 * (Lr + 1)); PIN(); }
      res[i % 3] = mfma(ring[i % 15], xf[i / 3], res[i % 3]);
      if (i + 15 < 54) ring[i % 15] = ofrag(i + 15);
      PINM();
    });
    const v2u xa = pk4(res[0]), xb = pk4(res[1]), xc = pk4(res[2]);
    if (!last) {
      const float sq = red_add(dot2(xa.x, dot2(xa.y, 0.f)) + dot2(xb.x, dot2(xb.y, 0.f))) + dot2(xc.x, dot2(xc.y, 0.f));
      gst128(rs, WS_FX + Lr * FRAG_BUF + T * FT + g * 1024 + l16, cat(xa, xb));
      gst128(rs, gq == 0 ? WS_FX + Lr * FRAG_BUF + T * FT + 16384 + g * 256 + tok * 16 : OOB, v4u{xc.x, xc.y, fu(sq), 0u});
      t_go = __builtin_amdgcn_s_memrealtime() + HOP_X;
#pragma unroll
      for (int i = 0; i < RING; ++i) ring[i] = wfrag(fr_slot(i));
      PIN();
      if (!old) sb_arrive(L, 1, lane);
      else {
#pragma unroll 1
        for (int f = w; f < 55; f += 4) dmaf(ws, nxtW, Lb, f, l16);
        if (first) {
#pragma unroll 1
          for (int f = 90 + w; f < Q_LATE; f += 4) dmaf(ws, nxtW, Lb, f, l16);
        }
      }
    } else {
      GAS unsigned char* orow = (GAS unsigned char*)out + (16 * T + tok) * 1152 + 72 * g;
      *(GAS v2u*)(orow + 8 * gq) = xa;
      *(GAS v2u*)(orow + 40 + 8 * gq) = xb;
      if (gq == 0) *(GAS v2u*)(orow + 32) = xc;
      if (b == 0 && tid == 0) __hip_atomic_store((GAS u64*)(ws + WS_EPOCH), tag, RLX_AGENT);
      DRAIN();
    }
  };
  layer(IC(0), IC(1), 0);
#pragma unroll 1
  for (int it = 0; it < 2; ++it) {
    layer(IC(1), IC(0), it);
    if (it == 0) layer(IC(0), IC(0), 1);
  }
}

extern "C" void kernel_launch(void* const* d_in, const int* in_sizes, int n_in, void* d_out, int out_size, void* d_ws, size_t ws_size, hipStream_t stream) {
  (void)hipFuncSetAttribute((const void*)fused_kernel, hipFuncAttributeMaxDynamicSharedMemorySize, L_TOTAL);
  fused_kernel<<<256, 512, L_TOTAL, stream>>>((const u16*)d_in[0], (const u16*)d_in[1], (const u16*)d_in[2], (const u16*)d_in[3],
                                              (const u16*)d_in[5], (const u16*)d_in[6], (u16*)d_out, (unsigned char*)d_ws);
}
```
